```python
import torch

def _softmax(x, axis=-1):
    return torch.softmax(x, dim=axis)
def _gelu(x):
    return torch.nn.functional.gelu(x, approximate="tanh")
def _silu(x):
    return torch.nn.functional.silu(x)
def _sigmoid(x):
    return torch.sigmoid(x)
def _layernorm(x, axis=-1, eps=1e-5):
    m = x.mean(dim=axis, keepdim=True)
    v = x.var(dim=axis, keepdim=True, unbiased=False)
    return (x - m) / torch.sqrt(v + eps)
def _scatter_add(tgt, idx, src, axis=0):
    return tgt.clone().index_add_(axis, idx, src)
def _segment_sum(src, seg, n):
    out = torch.zeros((n,) + src.shape[1:], dtype=src.dtype)
    return out.index_add_(0, seg, src)
def _pad(x, pw):
    flat = tuple(v for pair in reversed(pw) for v in pair)
    return torch.nn.functional.pad(x, flat)

kernel_name = 'standalone__gqa_softmax__near_ridge'

def setup_inputs():

    _w = (lambda a: (lambda t: t.to(torch.bfloat16) if t.is_floating_point() else t)(torch.as_tensor(a)))
    B, Hq, Hkv, Sq, Sk, D = 32, 32, 8, 368, 512, 128
    return {
        "q": _w((lambda *s: torch.randn(*s, dtype=torch.bfloat16) * 0.8)(B, Hq, Sq, D)),
        "k": _w((lambda *s: torch.randn(*s, dtype=torch.bfloat16) * 0.8)(B, Hkv, Sk, D)),
        "v": _w((lambda *s: torch.randn(*s, dtype=torch.bfloat16) * 0.05)(B, Hkv, Sk, D)),
    }

def reference(q, k, v):
    B, Hq, Sq, D = q.shape
    Hkv = k.shape[1]
    rep = Hq // Hkv
    k = (lambda x, n, axis: torch.repeat_interleave(x, n, dim=axis))(k, rep, axis=1)
    v = (lambda x, n, axis: torch.repeat_interleave(x, n, dim=axis))(v, rep, axis=1)
    scale = D ** -0.5
    scores = torch.einsum("bhqd,bhkd->bhqk", q, k) * scale
    p = _softmax(scores, axis=-1)
    return torch.einsum("bhqk,bhkd->bhqd", p, v)
```

```cpp
#include <hip/hip_runtime.h>
#include <hip/hip_bf16.h>

__global__ __launch_bounds__(512, 2) void standalone__gqa_softmax__near_ridge_kernel(
    const char* __restrict__ q, const char* __restrict__ k, const char* __restrict__ v, char* __restrict__ o) {
  __shared__ __attribute__((aligned(1024))) char lds[163840];
  const unsigned wave = __builtin_amdgcn_readfirstlane(threadIdx.x >> 6);
  const size_t unit = blockIdx.x;
  const char* qb = q + unit * 376832u;
  const char* kb = k + unit * 131072u;
  const char* vb = v + unit * 131072u;
  char* ob = o + unit * 376832u;
  const unsigned ldsb = __builtin_amdgcn_readfirstlane((unsigned)(size_t)lds);
  asm volatile(
    "s_mov_b32 s39, %5\n\t"
    "s_and_b32 s38, %4, 3\n\t"
    "v_mbcnt_lo_u32_b32 v0, -1, 0\n\t"
    "v_mbcnt_hi_u32_b32 v0, -1, v0\n\t"
    "s_cmp_ge_u32 %4, 4\n\t"
    "s_cbranch_scc1 LPV_%=\n\t"
    "s_mov_b64 s[40:41], %1\n\t"
    "s_and_b32 s41, s41, 0xffff\n\t"
    "s_mov_b32 s42, 131072\n\t"
    "s_mov_b32 s43, 0x20000\n\t"
    "s_barrier\n\t"
    "v_and_b32 v120, 15, v0\n\t"
    "v_lshrrev_b32 v121, 4, v0\n\t"
    "v_lshlrev_b32 v122, 8, v120\n\t"
    "v_lshl_add_u32 v122, v121, 4, v122\n\t"
    "s_lshl_b32 s56, s38, 15\n\t"
    "s_add_u32 s56, s56, 0\n\t"
    "buffer_load_dwordx4 a[0:3], v122, s[40:43], s56 offen offset:0\n\t"
    "buffer_load_dwordx4 a[4:7], v122, s[40:43], s56 offen offset:64\n\t"
    "buffer_load_dwordx4 a[8:11], v122, s[40:43], s56 offen offset:128\n\t"
    "buffer_load_dwordx4 a[12:15], v122, s[40:43], s56 offen offset:192\n\t"
    "s_lshl_b32 s56, s38, 15\n\t"
    "s_add_u32 s56, s56, 4096\n\t"
    "buffer_load_dwordx4 a[16:19], v122, s[40:43], s56 offen offset:0\n\t"
    "buffer_load_dwordx4 a[20:23], v122, s[40:43], s56 offen offset:64\n\t"
    "buffer_load_dwordx4 a[24:27], v122, s[40:43], s56 offen offset:128\n\t"
    "buffer_load_dwordx4 a[28:31], v122, s[40:43], s56 offen offset:192\n\t"
    "s_lshl_b32 s56, s38, 15\n\t"
    "s_add_u32 s56, s56, 8192\n\t"
    "buffer_load_dwordx4 a[32:35], v122, s[40:43], s56 offen offset:0\n\t"
    "buffer_load_dwordx4 a[36:39], v122, s[40:43], s56 offen offset:64\n\t"
    "buffer_load_dwordx4 a[40:43], v122, s[40:43], s56 offen offset:128\n\t"
    "buffer_load_dwordx4 a[44:47], v122, s[40:43], s56 offen offset:192\n\t"
    "s_lshl_b32 s56, s38, 15\n\t"
    "s_add_u32 s56, s56, 12288\n\t"
    "buffer_load_dwordx4 a[48:51], v122, s[40:43], s56 offen offset:0\n\t"
    "buffer_load_dwordx4 a[52:55], v122, s[40:43], s56 offen offset:64\n\t"
    "buffer_load_dwordx4 a[56:59], v122, s[40:43], s56 offen offset:128\n\t"
    "buffer_load_dwordx4 a[60:63], v122, s[40:43], s56 offen offset:192\n\t"
    "v_lshrrev_b32 v123, 3, v120\n\t"
    "v_lshlrev_b32 v123, 11, v123\n\t"
    "v_and_b32 v124, 7, v120\n\t"
    "v_lshl_add_u32 v123, v124, 6, v123\n\t"
    "v_lshrrev_b32 v124, 2, v120\n\t"
    "v_sub_u32 v124, 0, v124\n\t"
    "v_and_b32 v124, 3, v124\n\t"
    "v_xor_b32 v124, v124, v121\n\t"
    "v_lshl_add_u32 v123, v124, 4, v123\n\t"
    "s_add_u32 s57, s39, 65536\n\t"
    "v_add_u32 v117, s57, v123\n\t"
    "s_lshl_b32 s57, s38, 13\n\t"
    "s_add_u32 s57, s57, s39\n\t"
    "v_lshlrev_b32 v124, 4, v0\n\t"
    "v_add_u32 v118, s57, v124\n\t"
    "s_lshl_b32 s57, s38, 8\n\t"
    "s_add_u32 s57, s57, s39\n\t"
    "s_add_u32 s57, s57, 147456\n\t"
    "v_lshlrev_b32 v124, 2, v0\n\t"
    "v_add_u32 v119, s57, v124\n\t"
    "v_lshrrev_b32 v124, 5, v0\n\t"
    "v_lshl_add_u32 v124, v124, 4, v120\n\t"
    "v_lshlrev_b32 v124, 2, v124\n\t"
    "s_lshl_b32 s57, s38, 7\n\t"
    "s_add_u32 s57, s57, s39\n\t"
    "s_add_u32 s57, s57, 161792\n\t"
    "v_add_u32 v125, s57, v124\n\t"
    "s_mov_b32 s66, 0x3e0293ee\n\t"
    "v_mov_b32 v104, 0x3f803f80\n\t"
    "v_mov_b32 v105, 0x3f803f80\n\t"
    "v_mov_b32 v106, 0x3f803f80\n\t"
    "v_mov_b32 v107, 0x3f803f80\n\t"
    "s_add_u32 s56, s39, 149516\n\t"
    "v_mov_b32 v126, s56\n\t"
    "s_waitcnt vmcnt(15)\n\t"
    "v_accvgpr_read_b32 v50, a0\n\t"
    "v_lshlrev_b32 v48, 16, v50\n\t"
    "v_and_b32 v49, 0xffff0000, v50\n\t"
    "v_mul_f32 v48, s66, v48\n\t"
    "v_mul_f32 v49, s66, v49\n\t"
    "v_cvt_pk_bf16_f32 v50, v48, v49\n\t"
    "v_accvgpr_write_b32 a0, v50\n\t"
    "v_accvgpr_read_b32 v53, a1\n\t"
    "v_lshlrev_b32 v51, 16, v53\n\t"
    "v_and_b32 v52, 0xffff0000, v53\n\t"
    "v_mul_f32 v51, s66, v51\n\t"
    "v_mul_f32 v52, s66, v52\n\t"
    "v_cvt_pk_bf16_f32 v53, v51, v52\n\t"
    "v_accvgpr_write_b32 a1, v53\n\t"
    "v_accvgpr_read_b32 v56, a2\n\t"
    "v_lshlrev_b32 v54, 16, v56\n\t"
    "v_and_b32 v55, 0xffff0000, v56\n\t"
    "v_mul_f32 v54, s66, v54\n\t"
    "v_mul_f32 v55, s66, v55\n\t"
    "v_cvt_pk_bf16_f32 v56, v54, v55\n\t"
    "v_accvgpr_write_b32 a2, v56\n\t"
    "v_accvgpr_read_b32 v59, a3\n\t"
    "v_lshlrev_b32 v57, 16, v59\n\t"
    "v_and_b32 v58, 0xffff0000, v59\n\t"
    "v_mul_f32 v57, s66, v57\n\t"
    "v_mul_f32 v58, s66, v58\n\t"
    "v_cvt_pk_bf16_f32 v59, v57, v58\n\t"
    "v_accvgpr_write_b32 a3, v59\n\t"
    "s_waitcnt vmcnt(14)\n\t"
    "v_accvgpr_read_b32 v50, a4\n\t"
    "v_lshlrev_b32 v48, 16, v50\n\t"
    "v_and_b32 v49, 0xffff0000, v50\n\t"
    "v_mul_f32 v48, s66, v48\n\t"
    "v_mul_f32 v49, s66, v49\n\t"
    "v_cvt_pk_bf16_f32 v50, v48, v49\n\t"
    "v_accvgpr_write_b32 a4, v50\n\t"
    "v_accvgpr_read_b32 v53, a5\n\t"
    "v_lshlrev_b32 v51, 16, v53\n\t"
    "v_and_b32 v52, 0xffff0000, v53\n\t"
    "v_mul_f32 v51, s66, v51\n\t"
    "v_mul_f32 v52, s66, v52\n\t"
    "v_cvt_pk_bf16_f32 v53, v51, v52\n\t"
    "v_accvgpr_write_b32 a5, v53\n\t"
    "v_accvgpr_read_b32 v56, a6\n\t"
    "v_lshlrev_b32 v54, 16, v56\n\t"
    "v_and_b32 v55, 0xffff0000, v56\n\t"
    "v_mul_f32 v54, s66, v54\n\t"
    "v_mul_f32 v55, s66, v55\n\t"
    "v_cvt_pk_bf16_f32 v56, v54, v55\n\t"
    "v_accvgpr_write_b32 a6, v56\n\t"
    "v_accvgpr_read_b32 v59, a7\n\t"
    "v_lshlrev_b32 v57, 16, v59\n\t"
    "v_and_b32 v58, 0xffff0000, v59\n\t"
    "v_mul_f32 v57, s66, v57\n\t"
    "v_mul_f32 v58, s66, v58\n\t"
    "v_cvt_pk_bf16_f32 v59, v57, v58\n\t"
    "v_accvgpr_write_b32 a7, v59\n\t"
    "s_waitcnt vmcnt(13)\n\t"
    "v_accvgpr_read_b32 v50, a8\n\t"
    "v_lshlrev_b32 v48, 16, v50\n\t"
    "v_and_b32 v49, 0xffff0000, v50\n\t"
    "v_mul_f32 v48, s66, v48\n\t"
    "v_mul_f32 v49, s66, v49\n\t"
    "v_cvt_pk_bf16_f32 v50, v48, v49\n\t"
    "v_accvgpr_write_b32 a8, v50\n\t"
    "v_accvgpr_read_b32 v53, a9\n\t"
    "v_lshlrev_b32 v51, 16, v53\n\t"
    "v_and_b32 v52, 0xffff0000, v53\n\t"
    "v_mul_f32 v51, s66, v51\n\t"
    "v_mul_f32 v52, s66, v52\n\t"
    "v_cvt_pk_bf16_f32 v53, v51, v52\n\t"
    "v_accvgpr_write_b32 a9, v53\n\t"
    "v_accvgpr_read_b32 v56, a10\n\t"
    "v_lshlrev_b32 v54, 16, v56\n\t"
    "v_and_b32 v55, 0xffff0000, v56\n\t"
    "v_mul_f32 v54, s66, v54\n\t"
    "v_mul_f32 v55, s66, v55\n\t"
    "v_cvt_pk_bf16_f32 v56, v54, v55\n\t"
    "v_accvgpr_write_b32 a10, v56\n\t"
    "v_accvgpr_read_b32 v59, a11\n\t"
    "v_lshlrev_b32 v57, 16, v59\n\t"
    "v_and_b32 v58, 0xffff0000, v59\n\t"
    "v_mul_f32 v57, s66, v57\n\t"
    "v_mul_f32 v58, s66, v58\n\t"
    "v_cvt_pk_bf16_f32 v59, v57, v58\n\t"
    "v_accvgpr_write_b32 a11, v59\n\t"
    "s_waitcnt vmcnt(12)\n\t"
    "v_accvgpr_read_b32 v50, a12\n\t"
    "v_lshlrev_b32 v48, 16, v50\n\t"
    "v_and_b32 v49, 0xffff0000, v50\n\t"
    "v_mul_f32 v48, s66, v48\n\t"
    "v_mul_f32 v49, s66, v49\n\t"
    "v_cvt_pk_bf16_f32 v50, v48, v49\n\t"
    "v_accvgpr_write_b32 a12, v50\n\t"
    "v_accvgpr_read_b32 v53, a13\n\t"
    "v_lshlrev_b32 v51, 16, v53\n\t"
    "v_and_b32 v52, 0xffff0000, v53\n\t"
    "v_mul_f32 v51, s66, v51\n\t"
    "v_mul_f32 v52, s66, v52\n\t"
    "v_cvt_pk_bf16_f32 v53, v51, v52\n\t"
    "v_accvgpr_write_b32 a13, v53\n\t"
    "v_accvgpr_read_b32 v56, a14\n\t"
    "v_lshlrev_b32 v54, 16, v56\n\t"
    "v_and_b32 v55, 0xffff0000, v56\n\t"
    "v_mul_f32 v54, s66, v54\n\t"
    "v_mul_f32 v55, s66, v55\n\t"
    "v_cvt_pk_bf16_f32 v56, v54, v55\n\t"
    "v_accvgpr_write_b32 a14, v56\n\t"
    "v_accvgpr_read_b32 v59, a15\n\t"
    "v_lshlrev_b32 v57, 16, v59\n\t"
    "v_and_b32 v58, 0xffff0000, v59\n\t"
    "v_mul_f32 v57, s66, v57\n\t"
    "v_mul_f32 v58, s66, v58\n\t"
    "v_cvt_pk_bf16_f32 v59, v57, v58\n\t"
    "v_accvgpr_write_b32 a15, v59\n\t"
    "s_waitcnt vmcnt(11)\n\t"
    "v_accvgpr_read_b32 v50, a16\n\t"
    "v_lshlrev_b32 v48, 16, v50\n\t"
    "v_and_b32 v49, 0xffff0000, v50\n\t"
    "v_mul_f32 v48, s66, v48\n\t"
    "v_mul_f32 v49, s66, v49\n\t"
    "v_cvt_pk_bf16_f32 v50, v48, v49\n\t"
    "v_accvgpr_write_b32 a16, v50\n\t"
    "v_accvgpr_read_b32 v53, a17\n\t"
    "v_lshlrev_b32 v51, 16, v53\n\t"
    "v_and_b32 v52, 0xffff0000, v53\n\t"
    "v_mul_f32 v51, s66, v51\n\t"
    "v_mul_f32 v52, s66, v52\n\t"
    "v_cvt_pk_bf16_f32 v53, v51, v52\n\t"
    "v_accvgpr_write_b32 a17, v53\n\t"
    "v_accvgpr_read_b32 v56, a18\n\t"
    "v_lshlrev_b32 v54, 16, v56\n\t"
    "v_and_b32 v55, 0xffff0000, v56\n\t"
    "v_mul_f32 v54, s66, v54\n\t"
    "v_mul_f32 v55, s66, v55\n\t"
    "v_cvt_pk_bf16_f32 v56, v54, v55\n\t"
    "v_accvgpr_write_b32 a18, v56\n\t"
    "v_accvgpr_read_b32 v59, a19\n\t"
    "v_lshlrev_b32 v57, 16, v59\n\t"
    "v_and_b32 v58, 0xffff0000, v59\n\t"
    "v_mul_f32 v57, s66, v57\n\t"
    "v_mul_f32 v58, s66, v58\n\t"
    "v_cvt_pk_bf16_f32 v59, v57, v58\n\t"
    "v_accvgpr_write_b32 a19, v59\n\t"
    "s_waitcnt vmcnt(10)\n\t"
    "v_accvgpr_read_b32 v50, a20\n\t"
    "v_lshlrev_b32 v48, 16, v50\n\t"
    "v_and_b32 v49, 0xffff0000, v50\n\t"
    "v_mul_f32 v48, s66, v48\n\t"
    "v_mul_f32 v49, s66, v49\n\t"
    "v_cvt_pk_bf16_f32 v50, v48, v49\n\t"
    "v_accvgpr_write_b32 a20, v50\n\t"
    "v_accvgpr_read_b32 v53, a21\n\t"
    "v_lshlrev_b32 v51, 16, v53\n\t"
    "v_and_b32 v52, 0xffff0000, v53\n\t"
    "v_mul_f32 v51, s66, v51\n\t"
    "v_mul_f32 v52, s66, v52\n\t"
    "v_cvt_pk_bf16_f32 v53, v51, v52\n\t"
    "v_accvgpr_write_b32 a21, v53\n\t"
    "v_accvgpr_read_b32 v56, a22\n\t"
    "v_lshlrev_b32 v54, 16, v56\n\t"
    "v_and_b32 v55, 0xffff0000, v56\n\t"
    "v_mul_f32 v54, s66, v54\n\t"
    "v_mul_f32 v55, s66, v55\n\t"
    "v_cvt_pk_bf16_f32 v56, v54, v55\n\t"
    "v_accvgpr_write_b32 a22, v56\n\t"
    "v_accvgpr_read_b32 v59, a23\n\t"
    "v_lshlrev_b32 v57, 16, v59\n\t"
    "v_and_b32 v58, 0xffff0000, v59\n\t"
    "v_mul_f32 v57, s66, v57\n\t"
    "v_mul_f32 v58, s66, v58\n\t"
    "v_cvt_pk_bf16_f32 v59, v57, v58\n\t"
    "v_accvgpr_write_b32 a23, v59\n\t"
    "s_waitcnt vmcnt(9)\n\t"
    "v_accvgpr_read_b32 v50, a24\n\t"
    "v_lshlrev_b32 v48, 16, v50\n\t"
    "v_and_b32 v49, 0xffff0000, v50\n\t"
    "v_mul_f32 v48, s66, v48\n\t"
    "v_mul_f32 v49, s66, v49\n\t"
    "v_cvt_pk_bf16_f32 v50, v48, v49\n\t"
    "v_accvgpr_write_b32 a24, v50\n\t"
    "v_accvgpr_read_b32 v53, a25\n\t"
    "v_lshlrev_b32 v51, 16, v53\n\t"
    "v_and_b32 v52, 0xffff0000, v53\n\t"
    "v_mul_f32 v51, s66, v51\n\t"
    "v_mul_f32 v52, s66, v52\n\t"
    "v_cvt_pk_bf16_f32 v53, v51, v52\n\t"
    "v_accvgpr_write_b32 a25, v53\n\t"
    "v_accvgpr_read_b32 v56, a26\n\t"
    "v_lshlrev_b32 v54, 16, v56\n\t"
    "v_and_b32 v55, 0xffff0000, v56\n\t"
    "v_mul_f32 v54, s66, v54\n\t"
    "v_mul_f32 v55, s66, v55\n\t"
    "v_cvt_pk_bf16_f32 v56, v54, v55\n\t"
    "v_accvgpr_write_b32 a26, v56\n\t"
    "v_accvgpr_read_b32 v59, a27\n\t"
    "v_lshlrev_b32 v57, 16, v59\n\t"
    "v_and_b32 v58, 0xffff0000, v59\n\t"
    "v_mul_f32 v57, s66, v57\n\t"
    "v_mul_f32 v58, s66, v58\n\t"
    "v_cvt_pk_bf16_f32 v59, v57, v58\n\t"
    "v_accvgpr_write_b32 a27, v59\n\t"
    "s_waitcnt vmcnt(8)\n\t"
    "v_accvgpr_read_b32 v50, a28\n\t"
    "v_lshlrev_b32 v48, 16, v50\n\t"
    "v_and_b32 v49, 0xffff0000, v50\n\t"
    "v_mul_f32 v48, s66, v48\n\t"
    "v_mul_f32 v49, s66, v49\n\t"
    "v_cvt_pk_bf16_f32 v50, v48, v49\n\t"
    "v_accvgpr_write_b32 a28, v50\n\t"
    "v_accvgpr_read_b32 v53, a29\n\t"
    "v_lshlrev_b32 v51, 16, v53\n\t"
    "v_and_b32 v52, 0xffff0000, v53\n\t"
    "v_mul_f32 v51, s66, v51\n\t"
    "v_mul_f32 v52, s66, v52\n\t"
    "v_cvt_pk_bf16_f32 v53, v51, v52\n\t"
    "v_accvgpr_write_b32 a29, v53\n\t"
    "v_accvgpr_read_b32 v56, a30\n\t"
    "v_lshlrev_b32 v54, 16, v56\n\t"
    "v_and_b32 v55, 0xffff0000, v56\n\t"
    "v_mul_f32 v54, s66, v54\n\t"
    "v_mul_f32 v55, s66, v55\n\t"
    "v_cvt_pk_bf16_f32 v56, v54, v55\n\t"
    "v_accvgpr_write_b32 a30, v56\n\t"
    "v_accvgpr_read_b32 v59, a31\n\t"
    "v_lshlrev_b32 v57, 16, v59\n\t"
    "v_and_b32 v58, 0xffff0000, v59\n\t"
    "v_mul_f32 v57, s66, v57\n\t"
    "v_mul_f32 v58, s66, v58\n\t"
    "v_cvt_pk_bf16_f32 v59, v57, v58\n\t"
    "v_accvgpr_write_b32 a31, v59\n\t"
    "LQW_%=:\n\t"
    "ds_read_b32 v127, v126 offset:0\n\t"
    "s_waitcnt lgkmcnt(0)\n\t"
    "v_readfirstlane_b32 s57, v127\n\t"
    "s_nop 3\n\t"
    "s_cmp_ge_u32 s57, 4\n\t"
    "s_cbranch_scc0 LQW_%=\n\t"
    "ds_read_b128 v[0:3], v117 offset:0\n\t"
    "ds_read_b128 v[4:7], v117 offset:512\n\t"
    "ds_read_b128 v[8:11], v117 offset:1024\n\t"
    "ds_read_b128 v[12:15], v117 offset:1536\n\t"
    "ds_read_b128 v[16:19], v117 offset:4096\n\t"
    "ds_read_b128 v[20:23], v117 offset:4608\n\t"
    "ds_read_b128 v[24:27], v117 offset:5120\n\t"
    "ds_read_b128 v[28:31], v117 offset:5632\n\t"
    "s_waitcnt lgkmcnt(7)\n\t"
    "v_mfma_f32_16x16x32_bf16 v[32:35], a[0:3], v[0:3], 0\n\t"
    "s_waitcnt lgkmcnt(6)\n\t"
    "v_mfma_f32_16x16x32_bf16 v[32:35], a[4:7], v[4:7], v[32:35]\n\t"
    "s_waitcnt lgkmcnt(5)\n\t"
    "v_mfma_f32_16x16x32_bf16 v[32:35], a[8:11], v[8:11], v[32:35]\n\t"
    "s_waitcnt lgkmcnt(4)\n\t"
    "v_mfma_f32_16x16x32_bf16 v[32:35], a[12:15], v[12:15], v[32:35]\n\t"
    "v_mfma_f32_16x16x32_bf16 v[36:39], a[16:19], v[0:3], 0\n\t"
    "v_mfma_f32_16x16x32_bf16 v[36:39], a[20:23], v[4:7], v[36:39]\n\t"
    "v_mfma_f32_16x16x32_bf16 v[36:39], a[24:27], v[8:11], v[36:39]\n\t"
    "v_mfma_f32_16x16x32_bf16 v[36:39], a[28:31], v[12:15], v[36:39]\n\t"
    "ds_read_b128 v[0:3], v117 offset:8192\n\t"
    "ds_read_b128 v[4:7], v117 offset:8704\n\t"
    "ds_read_b128 v[8:11], v117 offset:9216\n\t"
    "ds_read_b128 v[12:15], v117 offset:9728\n\t"
    "s_waitcnt lgkmcnt(7)\n\t"
    "v_mfma_f32_16x16x32_bf16 v[40:43], a[0:3], v[16:19], 0\n\t"
    "v_exp_f32 v32, v32\n\t"
    "s_waitcnt lgkmcnt(6)\n\t"
    "v_mfma_f32_16x16x32_bf16 v[40:43], a[4:7], v[20:23], v[40:43]\n\t"
    "v_exp_f32 v33, v33\n\t"
    "s_waitcnt lgkmcnt(5)\n\t"
    "v_mfma_f32_16x16x32_bf16 v[40:43], a[8:11], v[24:27], v[40:43]\n\t"
    "v_exp_f32 v34, v34\n\t"
    "s_waitcnt lgkmcnt(4)\n\t"
    "v_mfma_f32_16x16x32_bf16 v[40:43], a[12:15], v[28:31], v[40:43]\n\t"
    "v_exp_f32 v35, v35\n\t"
    "v_mfma_f32_16x16x32_bf16 v[44:47], a[16:19], v[16:19], 0\n\t"
    "v_exp_f32 v36, v36\n\t"
    "v_mfma_f32_16x16x32_bf16 v[44:47], a[20:23], v[20:23], v[44:47]\n\t"
    "v_exp_f32 v37, v37\n\t"
    "v_mfma_f32_16x16x32_bf16 v[44:47], a[24:27], v[24:27], v[44:47]\n\t"
    "v_exp_f32 v38, v38\n\t"
    "v_mfma_f32_16x16x32_bf16 v[44:47], a[28:31], v[28:31], v[44:47]\n\t"
    "v_exp_f32 v39, v39\n\t"
    "v_cvt_pk_bf16_f32 v96, v32, v33\n\t"
    "v_cvt_pk_bf16_f32 v97, v34, v35\n\t"
    "v_cvt_pk_bf16_f32 v98, v36, v37\n\t"
    "v_cvt_pk_bf16_f32 v99, v38, v39\n\t"
    "ds_write_b128 v118, v[96:99] offset:0\n\t"
    "s_nop 0\n\t"
    "v_mfma_f32_16x16x32_bf16 v[64:67], v[104:107], v[96:99], 0\n\t"
    "ds_read_b128 v[16:19], v117 offset:12288\n\t"
    "ds_read_b128 v[20:23], v117 offset:12800\n\t"
    "ds_read_b128 v[24:27], v117 offset:13312\n\t"
    "ds_read_b128 v[28:31], v117 offset:13824\n\t"
    "s_waitcnt lgkmcnt(8)\n\t"
    "v_mfma_f32_16x16x32_bf16 v[32:35], a[0:3], v[0:3], 0\n\t"
    "v_exp_f32 v40, v40\n\t"
    "s_waitcnt lgkmcnt(7)\n\t"
    "v_mfma_f32_16x16x32_bf16 v[32:35], a[4:7], v[4:7], v[32:35]\n\t"
    "v_exp_f32 v41, v41\n\t"
    "s_waitcnt lgkmcnt(6)\n\t"
    "v_mfma_f32_16x16x32_bf16 v[32:35], a[8:11], v[8:11], v[32:35]\n\t"
    "v_exp_f32 v42, v42\n\t"
    "s_waitcnt lgkmcnt(5)\n\t"
    "v_mfma_f32_16x16x32_bf16 v[32:35], a[12:15], v[12:15], v[32:35]\n\t"
    "v_exp_f32 v43, v43\n\t"
    "v_mfma_f32_16x16x32_bf16 v[36:39], a[16:19], v[0:3], 0\n\t"
    "v_exp_f32 v44, v44\n\t"
    "v_mfma_f32_16x16x32_bf16 v[36:39], a[20:23], v[4:7], v[36:39]\n\t"
    "v_exp_f32 v45, v45\n\t"
    "v_mfma_f32_16x16x32_bf16 v[36:39], a[24:27], v[8:11], v[36:39]\n\t"
    "v_exp_f32 v46, v46\n\t"
    "v_mfma_f32_16x16x32_bf16 v[36:39], a[28:31], v[12:15], v[36:39]\n\t"
    "v_exp_f32 v47, v47\n\t"
    "v_cvt_pk_bf16_f32 v100, v40, v41\n\t"
    "v_cvt_pk_bf16_f32 v101, v42, v43\n\t"
    "v_cvt_pk_bf16_f32 v102, v44, v45\n\t"
    "v_cvt_pk_bf16_f32 v103, v46, v47\n\t"
    "ds_write_b128 v118, v[100:103] offset:1024\n\t"
    "s_nop 0\n\t"
    "v_mfma_f32_16x16x32_bf16 v[68:71], v[104:107], v[100:103], 0\n\t"
    "ds_read_b128 v[0:3], v117 offset:16384\n\t"
    "ds_read_b128 v[4:7], v117 offset:16896\n\t"
    "ds_read_b128 v[8:11], v117 offset:17408\n\t"
    "ds_read_b128 v[12:15], v117 offset:17920\n\t"
    "s_waitcnt lgkmcnt(8)\n\t"
    "v_mfma_f32_16x16x32_bf16 v[40:43], a[0:3], v[16:19], 0\n\t"
    "v_exp_f32 v32, v32\n\t"
    "s_waitcnt lgkmcnt(7)\n\t"
    "v_mfma_f32_16x16x32_bf16 v[40:43], a[4:7], v[20:23], v[40:43]\n\t"
    "v_exp_f32 v33, v33\n\t"
    "s_waitcnt lgkmcnt(6)\n\t"
    "v_mfma_f32_16x16x32_bf16 v[40:43], a[8:11], v[24:27], v[40:43]\n\t"
    "v_exp_f32 v34, v34\n\t"
    "s_waitcnt lgkmcnt(5)\n\t"
    "v_mfma_f32_16x16x32_bf16 v[40:43], a[12:15], v[28:31], v[40:43]\n\t"
    "v_exp_f32 v35, v35\n\t"
    "v_mfma_f32_16x16x32_bf16 v[44:47], a[16:19], v[16:19], 0\n\t"
    "v_exp_f32 v36, v36\n\t"
    "v_mfma_f32_16x16x32_bf16 v[44:47], a[20:23], v[20:23], v[44:47]\n\t"
    "v_exp_f32 v37, v37\n\t"
    "v_mfma_f32_16x16x32_bf16 v[44:47], a[24:27], v[24:27], v[44:47]\n\t"
    "v_exp_f32 v38, v38\n\t"
    "v_mfma_f32_16x16x32_bf16 v[44:47], a[28:31], v[28:31], v[44:47]\n\t"
    "v_exp_f32 v39, v39\n\t"
    "v_cvt_pk_bf16_f32 v96, v32, v33\n\t"
    "v_cvt_pk_bf16_f32 v97, v34, v35\n\t"
    "v_cvt_pk_bf16_f32 v98, v36, v37\n\t"
    "v_cvt_pk_bf16_f32 v99, v38, v39\n\t"
    "ds_write_b128 v118, v[96:99] offset:2048\n\t"
    "s_nop 0\n\t"
    "v_mfma_f32_16x16x32_bf16 v[72:75], v[104:107], v[96:99], 0\n\t"
    "ds_read_b128 v[16:19], v117 offset:20480\n\t"
    "ds_read_b128 v[20:23], v117 offset:20992\n\t"
    "ds_read_b128 v[24:27], v117 offset:21504\n\t"
    "ds_read_b128 v[28:31], v117 offset:22016\n\t"
    "s_waitcnt lgkmcnt(8)\n\t"
    "v_mfma_f32_16x16x32_bf16 v[32:35], a[0:3], v[0:3], 0\n\t"
    "v_exp_f32 v40, v40\n\t"
    "s_waitcnt lgkmcnt(7)\n\t"
    "v_mfma_f32_16x16x32_bf16 v[32:35], a[4:7], v[4:7], v[32:35]\n\t"
    "v_exp_f32 v41, v41\n\t"
    "s_waitcnt lgkmcnt(6)\n\t"
    "v_mfma_f32_16x16x32_bf16 v[32:35], a[8:11], v[8:11], v[32:35]\n\t"
    "v_exp_f32 v42, v42\n\t"
    "s_waitcnt lgkmcnt(5)\n\t"
    "v_mfma_f32_16x16x32_bf16 v[32:35], a[12:15], v[12:15], v[32:35]\n\t"
    "v_exp_f32 v43, v43\n\t"
    "v_mfma_f32_16x16x32_bf16 v[36:39], a[16:19], v[0:3], 0\n\t"
    "v_exp_f32 v44, v44\n\t"
    "v_mfma_f32_16x16x32_bf16 v[36:39], a[20:23], v[4:7], v[36:39]\n\t"
    "v_exp_f32 v45, v45\n\t"
    "v_mfma_f32_16x16x32_bf16 v[36:39], a[24:27], v[8:11], v[36:39]\n\t"
    "v_exp_f32 v46, v46\n\t"
    "v_mfma_f32_16x16x32_bf16 v[36:39], a[28:31], v[12:15], v[36:39]\n\t"
    "v_exp_f32 v47, v47\n\t"
    "v_cvt_pk_bf16_f32 v100, v40, v41\n\t"
    "v_cvt_pk_bf16_f32 v101, v42, v43\n\t"
    "v_cvt_pk_bf16_f32 v102, v44, v45\n\t"
    "v_cvt_pk_bf16_f32 v103, v46, v47\n\t"
    "ds_write_b128 v118, v[100:103] offset:3072\n\t"
    "s_nop 0\n\t"
    "v_mfma_f32_16x16x32_bf16 v[76:79], v[104:107], v[100:103], 0\n\t"
    "ds_read_b128 v[0:3], v117 offset:24576\n\t"
    "ds_read_b128 v[4:7], v117 offset:25088\n\t"
    "ds_read_b128 v[8:11], v117 offset:25600\n\t"
    "ds_read_b128 v[12:15], v117 offset:26112\n\t"
    "s_waitcnt lgkmcnt(8)\n\t"
    "v_mfma_f32_16x16x32_bf16 v[40:43], a[0:3], v[16:19], 0\n\t"
    "v_exp_f32 v32, v32\n\t"
    "s_waitcnt lgkmcnt(7)\n\t"
    "v_mfma_f32_16x16x32_bf16 v[40:43], a[4:7], v[20:23], v[40:43]\n\t"
    "v_exp_f32 v33, v33\n\t"
    "s_waitcnt lgkmcnt(6)\n\t"
    "v_mfma_f32_16x16x32_bf16 v[40:43], a[8:11], v[24:27], v[40:43]\n\t"
    "v_exp_f32 v34, v34\n\t"
    "s_waitcnt lgkmcnt(5)\n\t"
    "v_mfma_f32_16x16x32_bf16 v[40:43], a[12:15], v[28:31], v[40:43]\n\t"
    "v_exp_f32 v35, v35\n\t"
    "v_mfma_f32_16x16x32_bf16 v[44:47], a[16:19], v[16:19], 0\n\t"
    "v_exp_f32 v36, v36\n\t"
    "v_mfma_f32_16x16x32_bf16 v[44:47], a[20:23], v[20:23], v[44:47]\n\t"
    "v_exp_f32 v37, v37\n\t"
    "v_mfma_f32_16x16x32_bf16 v[44:47], a[24:27], v[24:27], v[44:47]\n\t"
    "v_exp_f32 v38, v38\n\t"
    "v_mfma_f32_16x16x32_bf16 v[44:47], a[28:31], v[28:31], v[44:47]\n\t"
    "v_exp_f32 v39, v39\n\t"
    "v_cvt_pk_bf16_f32 v96, v32, v33\n\t"
    "v_cvt_pk_bf16_f32 v97, v34, v35\n\t"
    "v_cvt_pk_bf16_f32 v98, v36, v37\n\t"
    "v_cvt_pk_bf16_f32 v99, v38, v39\n\t"
    "ds_write_b128 v118, v[96:99] offset:4096\n\t"
    "s_nop 0\n\t"
    "v_mfma_f32_16x16x32_bf16 v[80:83], v[104:107], v[96:99], 0\n\t"
    "ds_read_b128 v[16:19], v117 offset:28672\n\t"
    "ds_read_b128 v[20:23], v117 offset:29184\n\t"
    "ds_read_b128 v[24:27], v117 offset:29696\n\t"
    "ds_read_b128 v[28:31], v117 offset:30208\n\t"
    "s_waitcnt lgkmcnt(8)\n\t"
    "v_mfma_f32_16x16x32_bf16 v[32:35], a[0:3], v[0:3], 0\n\t"
    "v_exp_f32 v40, v40\n\t"
    "s_waitcnt lgkmcnt(7)\n\t"
    "v_mfma_f32_16x16x32_bf16 v[32:35], a[4:7], v[4:7], v[32:35]\n\t"
    "v_exp_f32 v41, v41\n\t"
    "s_waitcnt lgkmcnt(6)\n\t"
    "v_mfma_f32_16x16x32_bf16 v[32:35], a[8:11], v[8:11], v[32:35]\n\t"
    "v_exp_f32 v42, v42\n\t"
    "s_waitcnt lgkmcnt(5)\n\t"
    "v_mfma_f32_16x16x32_bf16 v[32:35], a[12:15], v[12:15], v[32:35]\n\t"
    "v_exp_f32 v43, v43\n\t"
    "v_mfma_f32_16x16x32_bf16 v[36:39], a[16:19], v[0:3], 0\n\t"
    "v_exp_f32 v44, v44\n\t"
    "v_mfma_f32_16x16x32_bf16 v[36:39], a[20:23], v[4:7], v[36:39]\n\t"
    "v_exp_f32 v45, v45\n\t"
    "v_mfma_f32_16x16x32_bf16 v[36:39], a[24:27], v[8:11], v[36:39]\n\t"
    "v_exp_f32 v46, v46\n\t"
    "v_mfma_f32_16x16x32_bf16 v[36:39], a[28:31], v[12:15], v[36:39]\n\t"
    "v_exp_f32 v47, v47\n\t"
    "v_cvt_pk_bf16_f32 v100, v40, v41\n\t"
    "v_cvt_pk_bf16_f32 v101, v42, v43\n\t"
    "v_cvt_pk_bf16_f32 v102, v44, v45\n\t"
    "v_cvt_pk_bf16_f32 v103, v46, v47\n\t"
    "ds_write_b128 v118, v[100:103] offset:5120\n\t"
    "s_nop 0\n\t"
    "v_mfma_f32_16x16x32_bf16 v[84:87], v[104:107], v[100:103], 0\n\t"
    "s_waitcnt lgkmcnt(4)\n\t"
    "v_mfma_f32_16x16x32_bf16 v[40:43], a[0:3], v[16:19], 0\n\t"
    "v_exp_f32 v32, v32\n\t"
    "s_waitcnt lgkmcnt(3)\n\t"
    "v_mfma_f32_16x16x32_bf16 v[40:43], a[4:7], v[20:23], v[40:43]\n\t"
    "v_exp_f32 v33, v33\n\t"
    "s_waitcnt lgkmcnt(2)\n\t"
    "v_mfma_f32_16x16x32_bf16 v[40:43], a[8:11], v[24:27], v[40:43]\n\t"
    "v_exp_f32 v34, v34\n\t"
    "s_waitcnt lgkmcnt(1)\n\t"
    "v_mfma_f32_16x16x32_bf16 v[40:43], a[12:15], v[28:31], v[40:43]\n\t"
    "v_exp_f32 v35, v35\n\t"
    "v_mfma_f32_16x16x32_bf16 v[44:47], a[16:19], v[16:19], 0\n\t"
    "v_exp_f32 v36, v36\n\t"
    "v_mfma_f32_16x16x32_bf16 v[44:47], a[20:23], v[20:23], v[44:47]\n\t"
    "v_exp_f32 v37, v37\n\t"
    "v_mfma_f32_16x16x32_bf16 v[44:47], a[24:27], v[24:27], v[44:47]\n\t"
    "v_exp_f32 v38, v38\n\t"
    "v_mfma_f32_16x16x32_bf16 v[44:47], a[28:31], v[28:31], v[44:47]\n\t"
    "v_exp_f32 v39, v39\n\t"
    "v_cvt_pk_bf16_f32 v96, v32, v33\n\t"
    "v_cvt_pk_bf16_f32 v97, v34, v35\n\t"
    "v_cvt_pk_bf16_f32 v98, v36, v37\n\t"
    "v_cvt_pk_bf16_f32 v99, v38, v39\n\t"
    "ds_write_b128 v118, v[96:99] offset:6144\n\t"
    "s_nop 0\n\t"
    "v_mfma_f32_16x16x32_bf16 v[88:91], v[104:107], v[96:99], 0\n\t"
    "v_exp_f32 v40, v40\n\t"
    "v_exp_f32 v41, v41\n\t"
    "v_exp_f32 v42, v42\n\t"
    "v_exp_f32 v43, v43\n\t"
    "v_exp_f32 v44, v44\n\t"
    "v_exp_f32 v45, v45\n\t"
    "v_exp_f32 v46, v46\n\t"
    "v_exp_f32 v47, v47\n\t"
    "v_cvt_pk_bf16_f32 v100, v40, v41\n\t"
    "v_cvt_pk_bf16_f32 v101, v42, v43\n\t"
    "v_cvt_pk_bf16_f32 v102, v44, v45\n\t"
    "v_cvt_pk_bf16_f32 v103, v46, v47\n\t"
    "ds_write_b128 v118, v[100:103] offset:7168\n\t"
    "s_nop 0\n\t"
    "v_mfma_f32_16x16x32_bf16 v[92:95], v[104:107], v[100:103], 0\n\t"
    "s_waitcnt lgkmcnt(0)\n\t"
    "s_barrier\n\t"
    "s_lshl_b32 s56, s38, 15\n\t"
    "s_add_u32 s56, s56, 16384\n\t"
    "buffer_load_dwordx4 a[64:67], v122, s[40:43], s56 offen offset:0\n\t"
    "buffer_load_dwordx4 a[68:71], v122, s[40:43], s56 offen offset:64\n\t"
    "buffer_load_dwordx4 a[72:75], v122, s[40:43], s56 offen offset:128\n\t"
    "buffer_load_dwordx4 a[76:79], v122, s[40:43], s56 offen offset:192\n\t"
    "s_lshl_b32 s56, s38, 15\n\t"
    "s_add_u32 s56, s56, 20480\n\t"
    "buffer_load_dwordx4 a[80:83], v122, s[40:43], s56 offen offset:0\n\t"
    "buffer_load_dwordx4 a[84:87], v122, s[40:43], s56 offen offset:64\n\t"
    "buffer_load_dwordx4 a[88:91], v122, s[40:43], s56 offen offset:128\n\t"
    "buffer_load_dwordx4 a[92:95], v122, s[40:43], s56 offen offset:192\n\t"
    "s_waitcnt vmcnt(15)\n\t"
    "v_accvgpr_read_b32 v50, a32\n\t"
    "v_lshlrev_b32 v48, 16, v50\n\t"
    "v_and_b32 v49, 0xffff0000, v50\n\t"
    "v_mul_f32 v48, s66, v48\n\t"
    "v_mul_f32 v49, s66, v49\n\t"
    "v_cvt_pk_bf16_f32 v50, v48, v49\n\t"
    "v_accvgpr_write_b32 a32, v50\n\t"
    "v_accvgpr_read_b32 v53, a33\n\t"
    "v_lshlrev_b32 v51, 16, v53\n\t"
    "v_and_b32 v52, 0xffff0000, v53\n\t"
    "v_mul_f32 v51, s66, v51\n\t"
    "v_mul_f32 v52, s66, v52\n\t"
    "v_cvt_pk_bf16_f32 v53, v51, v52\n\t"
    "v_accvgpr_write_b32 a33, v53\n\t"
    "v_accvgpr_read_b32 v56, a34\n\t"
    "v_lshlrev_b32 v54, 16, v56\n\t"
    "v_and_b32 v55, 0xffff0000, v56\n\t"
    "v_mul_f32 v54, s66, v54\n\t"
    "v_mul_f32 v55, s66, v55\n\t"
    "v_cvt_pk_bf16_f32 v56, v54, v55\n\t"
    "v_accvgpr_write_b32 a34, v56\n\t"
    "v_accvgpr_read_b32 v59, a35\n\t"
    "v_lshlrev_b32 v57, 16, v59\n\t"
    "v_and_b32 v58, 0xffff0000, v59\n\t"
    "v_mul_f32 v57, s66, v57\n\t"
    "v_mul_f32 v58, s66, v58\n\t"
    "v_cvt_pk_bf16_f32 v59, v57, v58\n\t"
    "v_accvgpr_write_b32 a35, v59\n\t"
    "s_waitcnt vmcnt(14)\n\t"
    "v_accvgpr_read_b32 v50, a36\n\t"
    "v_lshlrev_b32 v48, 16, v50\n\t"
    "v_and_b32 v49, 0xffff0000, v50\n\t"
    "v_mul_f32 v48, s66, v48\n\t"
    "v_mul_f32 v49, s66, v49\n\t"
    "v_cvt_pk_bf16_f32 v50, v48, v49\n\t"
    "v_accvgpr_write_b32 a36, v50\n\t"
    "v_accvgpr_read_b32 v53, a37\n\t"
    "v_lshlrev_b32 v51, 16, v53\n\t"
    "v_and_b32 v52, 0xffff0000, v53\n\t"
    "v_mul_f32 v51, s66, v51\n\t"
    "v_mul_f32 v52, s66, v52\n\t"
    "v_cvt_pk_bf16_f32 v53, v51, v52\n\t"
    "v_accvgpr_write_b32 a37, v53\n\t"
    "v_accvgpr_read_b32 v56, a38\n\t"
    "v_lshlrev_b32 v54, 16, v56\n\t"
    "v_and_b32 v55, 0xffff0000, v56\n\t"
    "v_mul_f32 v54, s66, v54\n\t"
    "v_mul_f32 v55, s66, v55\n\t"
    "v_cvt_pk_bf16_f32 v56, v54, v55\n\t"
    "v_accvgpr_write_b32 a38, v56\n\t"
    "v_accvgpr_read_b32 v59, a39\n\t"
    "v_lshlrev_b32 v57, 16, v59\n\t"
    "v_and_b32 v58, 0xffff0000, v59\n\t"
    "v_mul_f32 v57, s66, v57\n\t"
    "v_mul_f32 v58, s66, v58\n\t"
    "v_cvt_pk_bf16_f32 v59, v57, v58\n\t"
    "v_accvgpr_write_b32 a39, v59\n\t"
    "s_waitcnt vmcnt(13)\n\t"
    "v_accvgpr_read_b32 v50, a40\n\t"
    "v_lshlrev_b32 v48, 16, v50\n\t"
    "v_and_b32 v49, 0xffff0000, v50\n\t"
    "v_mul_f32 v48, s66, v48\n\t"
    "v_mul_f32 v49, s66, v49\n\t"
    "v_cvt_pk_bf16_f32 v50, v48, v49\n\t"
    "v_accvgpr_write_b32 a40, v50\n\t"
    "v_accvgpr_read_b32 v53, a41\n\t"
    "v_lshlrev_b32 v51, 16, v53\n\t"
    "v_and_b32 v52, 0xffff0000, v53\n\t"
    "v_mul_f32 v51, s66, v51\n\t"
    "v_mul_f32 v52, s66, v52\n\t"
    "v_cvt_pk_bf16_f32 v53, v51, v52\n\t"
    "v_accvgpr_write_b32 a41, v53\n\t"
    "v_accvgpr_read_b32 v56, a42\n\t"
    "v_lshlrev_b32 v54, 16, v56\n\t"
    "v_and_b32 v55, 0xffff0000, v56\n\t"
    "v_mul_f32 v54, s66, v54\n\t"
    "v_mul_f32 v55, s66, v55\n\t"
    "v_cvt_pk_bf16_f32 v56, v54, v55\n\t"
    "v_accvgpr_write_b32 a42, v56\n\t"
    "v_accvgpr_read_b32 v59, a43\n\t"
    "v_lshlrev_b32 v57, 16, v59\n\t"
    "v_and_b32 v58, 0xffff0000, v59\n\t"
    "v_mul_f32 v57, s66, v57\n\t"
    "v_mul_f32 v58, s66, v58\n\t"
    "v_cvt_pk_bf16_f32 v59, v57, v58\n\t"
    "v_accvgpr_write_b32 a43, v59\n\t"
    "s_waitcnt vmcnt(12)\n\t"
    "v_accvgpr_read_b32 v50, a44\n\t"
    "v_lshlrev_b32 v48, 16, v50\n\t"
    "v_and_b32 v49, 0xffff0000, v50\n\t"
    "v_mul_f32 v48, s66, v48\n\t"
    "v_mul_f32 v49, s66, v49\n\t"
    "v_cvt_pk_bf16_f32 v50, v48, v49\n\t"
    "v_accvgpr_write_b32 a44, v50\n\t"
    "v_accvgpr_read_b32 v53, a45\n\t"
    "v_lshlrev_b32 v51, 16, v53\n\t"
    "v_and_b32 v52, 0xffff0000, v53\n\t"
    "v_mul_f32 v51, s66, v51\n\t"
    "v_mul_f32 v52, s66, v52\n\t"
    "v_cvt_pk_bf16_f32 v53, v51, v52\n\t"
    "v_accvgpr_write_b32 a45, v53\n\t"
    "v_accvgpr_read_b32 v56, a46\n\t"
    "v_lshlrev_b32 v54, 16, v56\n\t"
    "v_and_b32 v55, 0xffff0000, v56\n\t"
    "v_mul_f32 v54, s66, v54\n\t"
    "v_mul_f32 v55, s66, v55\n\t"
    "v_cvt_pk_bf16_f32 v56, v54, v55\n\t"
    "v_accvgpr_write_b32 a46, v56\n\t"
    "v_accvgpr_read_b32 v59, a47\n\t"
    "v_lshlrev_b32 v57, 16, v59\n\t"
    "v_and_b32 v58, 0xffff0000, v59\n\t"
    "v_mul_f32 v57, s66, v57\n\t"
    "v_mul_f32 v58, s66, v58\n\t"
    "v_cvt_pk_bf16_f32 v59, v57, v58\n\t"
    "v_accvgpr_write_b32 a47, v59\n\t"
    "s_waitcnt vmcnt(11)\n\t"
    "v_accvgpr_read_b32 v50, a48\n\t"
    "v_lshlrev_b32 v48, 16, v50\n\t"
    "v_and_b32 v49, 0xffff0000, v50\n\t"
    "v_mul_f32 v48, s66, v48\n\t"
    "v_mul_f32 v49, s66, v49\n\t"
    "v_cvt_pk_bf16_f32 v50, v48, v49\n\t"
    "v_accvgpr_write_b32 a48, v50\n\t"
    "v_accvgpr_read_b32 v53, a49\n\t"
    "v_lshlrev_b32 v51, 16, v53\n\t"
    "v_and_b32 v52, 0xffff0000, v53\n\t"
    "v_mul_f32 v51, s66, v51\n\t"
    "v_mul_f32 v52, s66, v52\n\t"
    "v_cvt_pk_bf16_f32 v53, v51, v52\n\t"
    "v_accvgpr_write_b32 a49, v53\n\t"
    "v_accvgpr_read_b32 v56, a50\n\t"
    "v_lshlrev_b32 v54, 16, v56\n\t"
    "v_and_b32 v55, 0xffff0000, v56\n\t"
    "v_mul_f32 v54, s66, v54\n\t"
    "v_mul_f32 v55, s66, v55\n\t"
    "v_cvt_pk_bf16_f32 v56, v54, v55\n\t"
    "v_accvgpr_write_b32 a50, v56\n\t"
    "v_accvgpr_read_b32 v59, a51\n\t"
    "v_lshlrev_b32 v57, 16, v59\n\t"
    "v_and_b32 v58, 0xffff0000, v59\n\t"
    "v_mul_f32 v57, s66, v57\n\t"
    "v_mul_f32 v58, s66, v58\n\t"
    "v_cvt_pk_bf16_f32 v59, v57, v58\n\t"
    "v_accvgpr_write_b32 a51, v59\n\t"
    "s_waitcnt vmcnt(10)\n\t"
    "v_accvgpr_read_b32 v50, a52\n\t"
    "v_lshlrev_b32 v48, 16, v50\n\t"
    "v_and_b32 v49, 0xffff0000, v50\n\t"
    "v_mul_f32 v48, s66, v48\n\t"
    "v_mul_f32 v49, s66, v49\n\t"
    "v_cvt_pk_bf16_f32 v50, v48, v49\n\t"
    "v_accvgpr_write_b32 a52, v50\n\t"
    "v_accvgpr_read_b32 v53, a53\n\t"
    "v_lshlrev_b32 v51, 16, v53\n\t"
    "v_and_b32 v52, 0xffff0000, v53\n\t"
    "v_mul_f32 v51, s66, v51\n\t"
    "v_mul_f32 v52, s66, v52\n\t"
    "v_cvt_pk_bf16_f32 v53, v51, v52\n\t"
    "v_accvgpr_write_b32 a53, v53\n\t"
    "v_accvgpr_read_b32 v56, a54\n\t"
    "v_lshlrev_b32 v54, 16, v56\n\t"
    "v_and_b32 v55, 0xffff0000, v56\n\t"
    "v_mul_f32 v54, s66, v54\n\t"
    "v_mul_f32 v55, s66, v55\n\t"
    "v_cvt_pk_bf16_f32 v56, v54, v55\n\t"
    "v_accvgpr_write_b32 a54, v56\n\t"
    "v_accvgpr_read_b32 v59, a55\n\t"
    "v_lshlrev_b32 v57, 16, v59\n\t"
    "v_and_b32 v58, 0xffff0000, v59\n\t"
    "v_mul_f32 v57, s66, v57\n\t"
    "v_mul_f32 v58, s66, v58\n\t"
    "v_cvt_pk_bf16_f32 v59, v57, v58\n\t"
    "v_accvgpr_write_b32 a55, v59\n\t"
    "s_waitcnt vmcnt(9)\n\t"
    "v_accvgpr_read_b32 v50, a56\n\t"
    "v_lshlrev_b32 v48, 16, v50\n\t"
    "v_and_b32 v49, 0xffff0000, v50\n\t"
    "v_mul_f32 v48, s66, v48\n\t"
    "v_mul_f32 v49, s66, v49\n\t"
    "v_cvt_pk_bf16_f32 v50, v48, v49\n\t"
    "v_accvgpr_write_b32 a56, v50\n\t"
    "v_accvgpr_read_b32 v53, a57\n\t"
    "v_lshlrev_b32 v51, 16, v53\n\t"
    "v_and_b32 v52, 0xffff0000, v53\n\t"
    "v_mul_f32 v51, s66, v51\n\t"
    "v_mul_f32 v52, s66, v52\n\t"
    "v_cvt_pk_bf16_f32 v53, v51, v52\n\t"
    "v_accvgpr_write_b32 a57, v53\n\t"
    "v_accvgpr_read_b32 v56, a58\n\t"
    "v_lshlrev_b32 v54, 16, v56\n\t"
    "v_and_b32 v55, 0xffff0000, v56\n\t"
    "v_mul_f32 v54, s66, v54\n\t"
    "v_mul_f32 v55, s66, v55\n\t"
    "v_cvt_pk_bf16_f32 v56, v54, v55\n\t"
    "v_accvgpr_write_b32 a58, v56\n\t"
    "v_accvgpr_read_b32 v59, a59\n\t"
    "v_lshlrev_b32 v57, 16, v59\n\t"
    "v_and_b32 v58, 0xffff0000, v59\n\t"
    "v_mul_f32 v57, s66, v57\n\t"
    "v_mul_f32 v58, s66, v58\n\t"
    "v_cvt_pk_bf16_f32 v59, v57, v58\n\t"
    "v_accvgpr_write_b32 a59, v59\n\t"
    "s_waitcnt vmcnt(8)\n\t"
    "v_accvgpr_read_b32 v50, a60\n\t"
    "v_lshlrev_b32 v48, 16, v50\n\t"
    "v_and_b32 v49, 0xffff0000, v50\n\t"
    "v_mul_f32 v48, s66, v48\n\t"
    "v_mul_f32 v49, s66, v49\n\t"
    "v_cvt_pk_bf16_f32 v50, v48, v49\n\t"
    "v_accvgpr_write_b32 a60, v50\n\t"
    "v_accvgpr_read_b32 v53, a61\n\t"
    "v_lshlrev_b32 v51, 16, v53\n\t"
    "v_and_b32 v52, 0xffff0000, v53\n\t"
    "v_mul_f32 v51, s66, v51\n\t"
    "v_mul_f32 v52, s66, v52\n\t"
    "v_cvt_pk_bf16_f32 v53, v51, v52\n\t"
    "v_accvgpr_write_b32 a61, v53\n\t"
    "v_accvgpr_read_b32 v56, a62\n\t"
    "v_lshlrev_b32 v54, 16, v56\n\t"
    "v_and_b32 v55, 0xffff0000, v56\n\t"
    "v_mul_f32 v54, s66, v54\n\t"
    "v_mul_f32 v55, s66, v55\n\t"
    "v_cvt_pk_bf16_f32 v56, v54, v55\n\t"
    "v_accvgpr_write_b32 a62, v56\n\t"
    "v_accvgpr_read_b32 v59, a63\n\t"
    "v_lshlrev_b32 v57, 16, v59\n\t"
    "v_and_b32 v58, 0xffff0000, v59\n\t"
    "v_mul_f32 v57, s66, v57\n\t"
    "v_mul_f32 v58, s66, v58\n\t"
    "v_cvt_pk_bf16_f32 v59, v57, v58\n\t"
    "v_accvgpr_write_b32 a63, v59\n\t"
    "ds_read_b128 v[0:3], v117 offset:0\n\t"
    "ds_read_b128 v[4:7], v117 offset:512\n\t"
    "ds_read_b128 v[8:11], v117 offset:1024\n\t"
    "ds_read_b128 v[12:15], v117 offset:1536\n\t"
    "ds_read_b128 v[16:19], v117 offset:4096\n\t"
    "ds_read_b128 v[20:23], v117 offset:4608\n\t"
    "ds_read_b128 v[24:27], v117 offset:5120\n\t"
    "ds_read_b128 v[28:31], v117 offset:5632\n\t"
    "s_waitcnt lgkmcnt(7)\n\t"
    "v_mfma_f32_16x16x32_bf16 v[32:35], a[32:35], v[0:3], 0\n\t"
    "s_waitcnt lgkmcnt(6)\n\t"
    "v_mfma_f32_16x16x32_bf16 v[32:35], a[36:39], v[4:7], v[32:35]\n\t"
    "s_waitcnt lgkmcnt(5)\n\t"
    "v_mfma_f32_16x16x32_bf16 v[32:35], a[40:43], v[8:11], v[32:35]\n\t"
    "s_waitcnt lgkmcnt(4)\n\t"
    "v_mfma_f32_16x16x32_bf16 v[32:35], a[44:47], v[12:15], v[32:35]\n\t"
    "v_mfma_f32_16x16x32_bf16 v[36:39], a[48:51], v[0:3], 0\n\t"
    "v_mfma_f32_16x16x32_bf16 v[36:39], a[52:55], v[4:7], v[36:39]\n\t"
    "v_mfma_f32_16x16x32_bf16 v[36:39], a[56:59], v[8:11], v[36:39]\n\t"
    "v_mfma_f32_16x16x32_bf16 v[36:39], a[60:63], v[12:15], v[36:39]\n\t"
    "ds_read_b128 v[0:3], v117 offset:8192\n\t"
    "ds_read_b128 v[4:7], v117 offset:8704\n\t"
    "ds_read_b128 v[8:11], v117 offset:9216\n\t"
    "ds_read_b128 v[12:15], v117 offset:9728\n\t"
    "s_waitcnt lgkmcnt(7)\n\t"
    "v_mfma_f32_16x16x32_bf16 v[40:43], a[32:35], v[16:19], 0\n\t"
    "v_exp_f32 v32, v32\n\t"
    "s_waitcnt lgkmcnt(6)\n\t"
    "v_mfma_f32_16x16x32_bf16 v[40:43], a[36:39], v[20:23], v[40:43]\n\t"
    "v_exp_f32 v33, v33\n\t"
    "s_waitcnt lgkmcnt(5)\n\t"
    "v_mfma_f32_16x16x32_bf16 v[40:43], a[40:43], v[24:27], v[40:43]\n\t"
    "v_exp_f32 v34, v34\n\t"
    "s_waitcnt lgkmcnt(4)\n\t"
    "v_mfma_f32_16x16x32_bf16 v[40:43], a[44:47], v[28:31], v[40:43]\n\t"
    "v_exp_f32 v35, v35\n\t"
    "v_mfma_f32_16x16x32_bf16 v[44:47], a[48:51], v[16:19], 0\n\t"
    "v_exp_f32 v36, v36\n\t"
    "v_mfma_f32_16x16x32_bf16 v[44:47], a[52:55], v[20:23], v[44:47]\n\t"
    "v_exp_f32 v37, v37\n\t"
    "v_mfma_f32_16x16x32_bf16 v[44:47], a[56:59], v[24:27], v[44:47]\n\t"
    "v_exp_f32 v38, v38\n\t"
    "v_mfma_f32_16x16x32_bf16 v[44:47], a[60:63], v[28:31], v[44:47]\n\t"
    "v_exp_f32 v39, v39\n\t"
    "v_cvt_pk_bf16_f32 v96, v32, v33\n\t"
    "v_cvt_pk_bf16_f32 v97, v34, v35\n\t"
    "v_cvt_pk_bf16_f32 v98, v36, v37\n\t"
    "v_cvt_pk_bf16_f32 v99, v38, v39\n\t"
    "ds_write_b128 v118, v[96:99] offset:32768\n\t"
    "s_nop 0\n\t"
    "v_mfma_f32_16x16x32_bf16 v[64:67], v[104:107], v[96:99], v[64:67]\n\t"
    "ds_read_b128 v[16:19], v117 offset:12288\n\t"
    "ds_read_b128 v[20:23], v117 offset:12800\n\t"
    "ds_read_b128 v[24:27], v117 offset:13312\n\t"
    "ds_read_b128 v[28:31], v117 offset:13824\n\t"
    "s_waitcnt lgkmcnt(8)\n\t"
    "v_mfma_f32_16x16x32_bf16 v[32:35], a[32:35], v[0:3], 0\n\t"
    "v_exp_f32 v40, v40\n\t"
    "s_waitcnt lgkmcnt(7)\n\t"
    "v_mfma_f32_16x16x32_bf16 v[32:35], a[36:39], v[4:7], v[32:35]\n\t"
    "v_exp_f32 v41, v41\n\t"
    "s_waitcnt lgkmcnt(6)\n\t"
    "v_mfma_f32_16x16x32_bf16 v[32:35], a[40:43], v[8:11], v[32:35]\n\t"
    "v_exp_f32 v42, v42\n\t"
    "s_waitcnt lgkmcnt(5)\n\t"
    "v_mfma_f32_16x16x32_bf16 v[32:35], a[44:47], v[12:15], v[32:35]\n\t"
    "v_exp_f32 v43, v43\n\t"
    "v_mfma_f32_16x16x32_bf16 v[36:39], a[48:51], v[0:3], 0\n\t"
    "v_exp_f32 v44, v44\n\t"
    "v_mfma_f32_16x16x32_bf16 v[36:39], a[52:55], v[4:7], v[36:39]\n\t"
    "v_exp_f32 v45, v45\n\t"
    "v_mfma_f32_16x16x32_bf16 v[36:39], a[56:59], v[8:11], v[36:39]\n\t"
    "v_exp_f32 v46, v46\n\t"
    "v_mfma_f32_16x16x32_bf16 v[36:39], a[60:63], v[12:15], v[36:39]\n\t"
    "v_exp_f32 v47, v47\n\t"
    "v_cvt_pk_bf16_f32 v100, v40, v41\n\t"
    "v_cvt_pk_bf16_f32 v101, v42, v43\n\t"
    "v_cvt_pk_bf16_f32 v102, v44, v45\n\t"
    "v_cvt_pk_bf16_f32 v103, v46, v47\n\t"
    "ds_write_b128 v118, v[100:103] offset:33792\n\t"
    "s_nop 0\n\t"
    "v_mfma_f32_16x16x32_bf16 v[68:71], v[104:107], v[100:103], v[68:71]\n\t"
    "ds_read_b128 v[0:3], v117 offset:16384\n\t"
    "ds_read_b128 v[4:7], v117 offset:16896\n\t"
    "ds_read_b128 v[8:11], v117 offset:17408\n\t"
    "ds_read_b128 v[12:15], v117 offset:17920\n\t"
    "s_waitcnt lgkmcnt(8)\n\t"
    "v_mfma_f32_16x16x32_bf16 v[40:43], a[32:35], v[16:19], 0\n\t"
    "v_exp_f32 v32, v32\n\t"
    "s_waitcnt lgkmcnt(7)\n\t"
    "v_mfma_f32_16x16x32_bf16 v[40:43], a[36:39], v[20:23], v[40:43]\n\t"
    "v_exp_f32 v33, v33\n\t"
    "s_waitcnt lgkmcnt(6)\n\t"
    "v_mfma_f32_16x16x32_bf16 v[40:43], a[40:43], v[24:27], v[40:43]\n\t"
    "v_exp_f32 v34, v34\n\t"
    "s_waitcnt lgkmcnt(5)\n\t"
    "v_mfma_f32_16x16x32_bf16 v[40:43], a[44:47], v[28:31], v[40:43]\n\t"
    "v_exp_f32 v35, v35\n\t"
    "v_mfma_f32_16x16x32_bf16 v[44:47], a[48:51], v[16:19], 0\n\t"
    "v_exp_f32 v36, v36\n\t"
    "v_mfma_f32_16x16x32_bf16 v[44:47], a[52:55], v[20:23], v[44:47]\n\t"
    "v_exp_f32 v37, v37\n\t"
    "v_mfma_f32_16x16x32_bf16 v[44:47], a[56:59], v[24:27], v[44:47]\n\t"
    "v_exp_f32 v38, v38\n\t"
    "v_mfma_f32_16x16x32_bf16 v[44:47], a[60:63], v[28:31], v[44:47]\n\t"
    "v_exp_f32 v39, v39\n\t"
    "v_cvt_pk_bf16_f32 v96, v32, v33\n\t"
    "v_cvt_pk_bf16_f32 v97, v34, v35\n\t"
    "v_cvt_pk_bf16_f32 v98, v36, v37\n\t"
    "v_cvt_pk_bf16_f32 v99, v38, v39\n\t"
    "ds_write_b128 v118, v[96:99] offset:34816\n\t"
    "s_nop 0\n\t"
    "v_mfma_f32_16x16x32_bf16 v[72:75], v[104:107], v[96:99], v[72:75]\n\t"
    "ds_read_b128 v[16:19], v117 offset:20480\n\t"
    "ds_read_b128 v[20:23], v117 offset:20992\n\t"
    "ds_read_b128 v[24:27], v117 offset:21504\n\t"
    "ds_read_b128 v[28:31], v117 offset:22016\n\t"
    "s_waitcnt lgkmcnt(8)\n\t"
    "v_mfma_f32_16x16x32_bf16 v[32:35], a[32:35], v[0:3], 0\n\t"
    "v_exp_f32 v40, v40\n\t"
    "s_waitcnt lgkmcnt(7)\n\t"
    "v_mfma_f32_16x16x32_bf16 v[32:35], a[36:39], v[4:7], v[32:35]\n\t"
    "v_exp_f32 v41, v41\n\t"
    "s_waitcnt lgkmcnt(6)\n\t"
    "v_mfma_f32_16x16x32_bf16 v[32:35], a[40:43], v[8:11], v[32:35]\n\t"
    "v_exp_f32 v42, v42\n\t"
    "s_waitcnt lgkmcnt(5)\n\t"
    "v_mfma_f32_16x16x32_bf16 v[32:35], a[44:47], v[12:15], v[32:35]\n\t"
    "v_exp_f32 v43, v43\n\t"
    "v_mfma_f32_16x16x32_bf16 v[36:39], a[48:51], v[0:3], 0\n\t"
    "v_exp_f32 v44, v44\n\t"
    "v_mfma_f32_16x16x32_bf16 v[36:39], a[52:55], v[4:7], v[36:39]\n\t"
    "v_exp_f32 v45, v45\n\t"
    "v_mfma_f32_16x16x32_bf16 v[36:39], a[56:59], v[8:11], v[36:39]\n\t"
    "v_exp_f32 v46, v46\n\t"
    "v_mfma_f32_16x16x32_bf16 v[36:39], a[60:63], v[12:15], v[36:39]\n\t"
    "v_exp_f32 v47, v47\n\t"
    "v_cvt_pk_bf16_f32 v100, v40, v41\n\t"
    "v_cvt_pk_bf16_f32 v101, v42, v43\n\t"
    "v_cvt_pk_bf16_f32 v102, v44, v45\n\t"
    "v_cvt_pk_bf16_f32 v103, v46, v47\n\t"
    "ds_write_b128 v118, v[100:103] offset:35840\n\t"
    "s_nop 0\n\t"
    "v_mfma_f32_16x16x32_bf16 v[76:79], v[104:107], v[100:103], v[76:79]\n\t"
    "ds_read_b128 v[0:3], v117 offset:24576\n\t"
    "ds_read_b128 v[4:7], v117 offset:25088\n\t"
    "ds_read_b128 v[8:11], v117 offset:25600\n\t"
    "ds_read_b128 v[12:15], v117 offset:26112\n\t"
    "s_waitcnt lgkmcnt(8)\n\t"
    "v_mfma_f32_16x16x32_bf16 v[40:43], a[32:35], v[16:19], 0\n\t"
    "v_exp_f32 v32, v32\n\t"
    "s_waitcnt lgkmcnt(7)\n\t"
    "v_mfma_f32_16x16x32_bf16 v[40:43], a[36:39], v[20:23], v[40:43]\n\t"
    "v_exp_f32 v33, v33\n\t"
    "s_waitcnt lgkmcnt(6)\n\t"
    "v_mfma_f32_16x16x32_bf16 v[40:43], a[40:43], v[24:27], v[40:43]\n\t"
    "v_exp_f32 v34, v34\n\t"
    "s_waitcnt lgkmcnt(5)\n\t"
    "v_mfma_f32_16x16x32_bf16 v[40:43], a[44:47], v[28:31], v[40:43]\n\t"
    "v_exp_f32 v35, v35\n\t"
    "v_mfma_f32_16x16x32_bf16 v[44:47], a[48:51], v[16:19], 0\n\t"
    "v_exp_f32 v36, v36\n\t"
    "v_mfma_f32_16x16x32_bf16 v[44:47], a[52:55], v[20:23], v[44:47]\n\t"
    "v_exp_f32 v37, v37\n\t"
    "v_mfma_f32_16x16x32_bf16 v[44:47], a[56:59], v[24:27], v[44:47]\n\t"
    "v_exp_f32 v38, v38\n\t"
    "v_mfma_f32_16x16x32_bf16 v[44:47], a[60:63], v[28:31], v[44:47]\n\t"
    "v_exp_f32 v39, v39\n\t"
    "v_cvt_pk_bf16_f32 v96, v32, v33\n\t"
    "v_cvt_pk_bf16_f32 v97, v34, v35\n\t"
    "v_cvt_pk_bf16_f32 v98, v36, v37\n\t"
    "v_cvt_pk_bf16_f32 v99, v38, v39\n\t"
    "ds_write_b128 v118, v[96:99] offset:36864\n\t"
    "s_nop 0\n\t"
    "v_mfma_f32_16x16x32_bf16 v[80:83], v[104:107], v[96:99], v[80:83]\n\t"
    "ds_read_b128 v[16:19], v117 offset:28672\n\t"
    "ds_read_b128 v[20:23], v117 offset:29184\n\t"
    "ds_read_b128 v[24:27], v117 offset:29696\n\t"
    "ds_read_b128 v[28:31], v117 offset:30208\n\t"
    "s_waitcnt lgkmcnt(8)\n\t"
    "v_mfma_f32_16x16x32_bf16 v[32:35], a[32:35], v[0:3], 0\n\t"
    "v_exp_f32 v40, v40\n\t"
    "s_waitcnt lgkmcnt(7)\n\t"
    "v_mfma_f32_16x16x32_bf16 v[32:35], a[36:39], v[4:7], v[32:35]\n\t"
    "v_exp_f32 v41, v41\n\t"
    "s_waitcnt lgkmcnt(6)\n\t"
    "v_mfma_f32_16x16x32_bf16 v[32:35], a[40:43], v[8:11], v[32:35]\n\t"
    "v_exp_f32 v42, v42\n\t"
    "s_waitcnt lgkmcnt(5)\n\t"
    "v_mfma_f32_16x16x32_bf16 v[32:35], a[44:47], v[12:15], v[32:35]\n\t"
    "v_exp_f32 v43, v43\n\t"
    "v_mfma_f32_16x16x32_bf16 v[36:39], a[48:51], v[0:3], 0\n\t"
    "v_exp_f32 v44, v44\n\t"
    "v_mfma_f32_16x16x32_bf16 v[36:39], a[52:55], v[4:7], v[36:39]\n\t"
    "v_exp_f32 v45, v45\n\t"
    "v_mfma_f32_16x16x32_bf16 v[36:39], a[56:59], v[8:11], v[36:39]\n\t"
    "v_exp_f32 v46, v46\n\t"
    "v_mfma_f32_16x16x32_bf16 v[36:39], a[60:63], v[12:15], v[36:39]\n\t"
    "v_exp_f32 v47, v47\n\t"
    "v_cvt_pk_bf16_f32 v100, v40, v41\n\t"
    "v_cvt_pk_bf16_f32 v101, v42, v43\n\t"
    "v_cvt_pk_bf16_f32 v102, v44, v45\n\t"
    "v_cvt_pk_bf16_f32 v103, v46, v47\n\t"
    "ds_write_b128 v118, v[100:103] offset:37888\n\t"
    "s_nop 0\n\t"
    "v_mfma_f32_16x16x32_bf16 v[84:87], v[104:107], v[100:103], v[84:87]\n\t"
    "s_waitcnt lgkmcnt(4)\n\t"
    "v_mfma_f32_16x16x32_bf16 v[40:43], a[32:35], v[16:19], 0\n\t"
    "v_exp_f32 v32, v32\n\t"
    "s_waitcnt lgkmcnt(3)\n\t"
    "v_mfma_f32_16x16x32_bf16 v[40:43], a[36:39], v[20:23], v[40:43]\n\t"
    "v_exp_f32 v33, v33\n\t"
    "s_waitcnt lgkmcnt(2)\n\t"
    "v_mfma_f32_16x16x32_bf16 v[40:43], a[40:43], v[24:27], v[40:43]\n\t"
    "v_exp_f32 v34, v34\n\t"
    "s_waitcnt lgkmcnt(1)\n\t"
    "v_mfma_f32_16x16x32_bf16 v[40:43], a[44:47], v[28:31], v[40:43]\n\t"
    "v_exp_f32 v35, v35\n\t"
    "v_mfma_f32_16x16x32_bf16 v[44:47], a[48:51], v[16:19], 0\n\t"
    "v_exp_f32 v36, v36\n\t"
    "v_mfma_f32_16x16x32_bf16 v[44:47], a[52:55], v[20:23], v[44:47]\n\t"
    "v_exp_f32 v37, v37\n\t"
    "v_mfma_f32_16x16x32_bf16 v[44:47], a[56:59], v[24:27], v[44:47]\n\t"
    "v_exp_f32 v38, v38\n\t"
    "v_mfma_f32_16x16x32_bf16 v[44:47], a[60:63], v[28:31], v[44:47]\n\t"
    "v_exp_f32 v39, v39\n\t"
    "v_cvt_pk_bf16_f32 v96, v32, v33\n\t"
    "v_cvt_pk_bf16_f32 v97, v34, v35\n\t"
    "v_cvt_pk_bf16_f32 v98, v36, v37\n\t"
    "v_cvt_pk_bf16_f32 v99, v38, v39\n\t"
    "ds_write_b128 v118, v[96:99] offset:38912\n\t"
    "s_nop 0\n\t"
    "v_mfma_f32_16x16x32_bf16 v[88:91], v[104:107], v[96:99], v[88:91]\n\t"
    "v_exp_f32 v40, v40\n\t"
    "v_exp_f32 v41, v41\n\t"
    "v_exp_f32 v42, v42\n\t"
    "v_exp_f32 v43, v43\n\t"
    "v_exp_f32 v44, v44\n\t"
    "v_exp_f32 v45, v45\n\t"
    "v_exp_f32 v46, v46\n\t"
    "v_exp_f32 v47, v47\n\t"
    "v_cvt_pk_bf16_f32 v100, v40, v41\n\t"
    "v_cvt_pk_bf16_f32 v101, v42, v43\n\t"
    "v_cvt_pk_bf16_f32 v102, v44, v45\n\t"
    "v_cvt_pk_bf16_f32 v103, v46, v47\n\t"
    "ds_write_b128 v118, v[100:103] offset:39936\n\t"
    "s_nop 0\n\t"
    "v_mfma_f32_16x16x32_bf16 v[92:95], v[104:107], v[100:103], v[92:95]\n\t"
    "s_waitcnt lgkmcnt(0)\n\t"
    "s_barrier\n\t"
    "s_lshl_b32 s56, s38, 15\n\t"
    "s_add_u32 s56, s56, 24576\n\t"
    "buffer_load_dwordx4 a[96:99], v122, s[40:43], s56 offen offset:0\n\t"
    "buffer_load_dwordx4 a[100:103], v122, s[40:43], s56 offen offset:64\n\t"
    "buffer_load_dwordx4 a[104:107], v122, s[40:43], s56 offen offset:128\n\t"
    "buffer_load_dwordx4 a[108:111], v122, s[40:43], s56 offen offset:192\n\t"
    "s_lshl_b32 s56, s38, 15\n\t"
    "s_add_u32 s56, s56, 28672\n\t"
    "buffer_load_dwordx4 a[112:115], v122, s[40:43], s56 offen offset:0\n\t"
    "buffer_load_dwordx4 a[116:119], v122, s[40:43], s56 offen offset:64\n\t"
    "buffer_load_dwordx4 a[120:123], v122, s[40:43], s56 offen offset:128\n\t"
    "buffer_load_dwordx4 a[124:127], v122, s[40:43], s56 offen offset:192\n\t"
    "s_waitcnt vmcnt(15)\n\t"
    "v_accvgpr_read_b32 v50, a64\n\t"
    "v_lshlrev_b32 v48, 16, v50\n\t"
    "v_and_b32 v49, 0xffff0000, v50\n\t"
    "v_mul_f32 v48, s66, v48\n\t"
    "v_mul_f32 v49, s66, v49\n\t"
    "v_cvt_pk_bf16_f32 v50, v48, v49\n\t"
    "v_accvgpr_write_b32 a64, v50\n\t"
    "v_accvgpr_read_b32 v53, a65\n\t"
    "v_lshlrev_b32 v51, 16, v53\n\t"
    "v_and_b32 v52, 0xffff0000, v53\n\t"
    "v_mul_f32 v51, s66, v51\n\t"
    "v_mul_f32 v52, s66, v52\n\t"
    "v_cvt_pk_bf16_f32 v53, v51, v52\n\t"
    "v_accvgpr_write_b32 a65, v53\n\t"
    "v_accvgpr_read_b32 v56, a66\n\t"
    "v_lshlrev_b32 v54, 16, v56\n\t"
    "v_and_b32 v55, 0xffff0000, v56\n\t"
    "v_mul_f32 v54, s66, v54\n\t"
    "v_mul_f32 v55, s66, v55\n\t"
    "v_cvt_pk_bf16_f32 v56, v54, v55\n\t"
    "v_accvgpr_write_b32 a66, v56\n\t"
    "v_accvgpr_read_b32 v59, a67\n\t"
    "v_lshlrev_b32 v57, 16, v59\n\t"
    "v_and_b32 v58, 0xffff0000, v59\n\t"
    "v_mul_f32 v57, s66, v57\n\t"
    "v_mul_f32 v58, s66, v58\n\t"
    "v_cvt_pk_bf16_f32 v59, v57, v58\n\t"
    "v_accvgpr_write_b32 a67, v59\n\t"
    "s_waitcnt vmcnt(14)\n\t"
    "v_accvgpr_read_b32 v50, a68\n\t"
    "v_lshlrev_b32 v48, 16, v50\n\t"
    "v_and_b32 v49, 0xffff0000, v50\n\t"
    "v_mul_f32 v48, s66, v48\n\t"
    "v_mul_f32 v49, s66, v49\n\t"
    "v_cvt_pk_bf16_f32 v50, v48, v49\n\t"
    "v_accvgpr_write_b32 a68, v50\n\t"
    "v_accvgpr_read_b32 v53, a69\n\t"
    "v_lshlrev_b32 v51, 16, v53\n\t"
    "v_and_b32 v52, 0xffff0000, v53\n\t"
    "v_mul_f32 v51, s66, v51\n\t"
    "v_mul_f32 v52, s66, v52\n\t"
    "v_cvt_pk_bf16_f32 v53, v51, v52\n\t"
    "v_accvgpr_write_b32 a69, v53\n\t"
    "v_accvgpr_read_b32 v56, a70\n\t"
    "v_lshlrev_b32 v54, 16, v56\n\t"
    "v_and_b32 v55, 0xffff0000, v56\n\t"
    "v_mul_f32 v54, s66, v54\n\t"
    "v_mul_f32 v55, s66, v55\n\t"
    "v_cvt_pk_bf16_f32 v56, v54, v55\n\t"
    "v_accvgpr_write_b32 a70, v56\n\t"
    "v_accvgpr_read_b32 v59, a71\n\t"
    "v_lshlrev_b32 v57, 16, v59\n\t"
    "v_and_b32 v58, 0xffff0000, v59\n\t"
    "v_mul_f32 v57, s66, v57\n\t"
    "v_mul_f32 v58, s66, v58\n\t"
    "v_cvt_pk_bf16_f32 v59, v57, v58\n\t"
    "v_accvgpr_write_b32 a71, v59\n\t"
    "s_waitcnt vmcnt(13)\n\t"
    "v_accvgpr_read_b32 v50, a72\n\t"
    "v_lshlrev_b32 v48, 16, v50\n\t"
    "v_and_b32 v49, 0xffff0000, v50\n\t"
    "v_mul_f32 v48, s66, v48\n\t"
    "v_mul_f32 v49, s66, v49\n\t"
    "v_cvt_pk_bf16_f32 v50, v48, v49\n\t"
    "v_accvgpr_write_b32 a72, v50\n\t"
    "v_accvgpr_read_b32 v53, a73\n\t"
    "v_lshlrev_b32 v51, 16, v53\n\t"
    "v_and_b32 v52, 0xffff0000, v53\n\t"
    "v_mul_f32 v51, s66, v51\n\t"
    "v_mul_f32 v52, s66, v52\n\t"
    "v_cvt_pk_bf16_f32 v53, v51, v52\n\t"
    "v_accvgpr_write_b32 a73, v53\n\t"
    "v_accvgpr_read_b32 v56, a74\n\t"
    "v_lshlrev_b32 v54, 16, v56\n\t"
    "v_and_b32 v55, 0xffff0000, v56\n\t"
    "v_mul_f32 v54, s66, v54\n\t"
    "v_mul_f32 v55, s66, v55\n\t"
    "v_cvt_pk_bf16_f32 v56, v54, v55\n\t"
    "v_accvgpr_write_b32 a74, v56\n\t"
    "v_accvgpr_read_b32 v59, a75\n\t"
    "v_lshlrev_b32 v57, 16, v59\n\t"
    "v_and_b32 v58, 0xffff0000, v59\n\t"
    "v_mul_f32 v57, s66, v57\n\t"
    "v_mul_f32 v58, s66, v58\n\t"
    "v_cvt_pk_bf16_f32 v59, v57, v58\n\t"
    "v_accvgpr_write_b32 a75, v59\n\t"
    "s_waitcnt vmcnt(12)\n\t"
    "v_accvgpr_read_b32 v50, a76\n\t"
    "v_lshlrev_b32 v48, 16, v50\n\t"
    "v_and_b32 v49, 0xffff0000, v50\n\t"
    "v_mul_f32 v48, s66, v48\n\t"
    "v_mul_f32 v49, s66, v49\n\t"
    "v_cvt_pk_bf16_f32 v50, v48, v49\n\t"
    "v_accvgpr_write_b32 a76, v50\n\t"
    "v_accvgpr_read_b32 v53, a77\n\t"
    "v_lshlrev_b32 v51, 16, v53\n\t"
    "v_and_b32 v52, 0xffff0000, v53\n\t"
    "v_mul_f32 v51, s66, v51\n\t"
    "v_mul_f32 v52, s66, v52\n\t"
    "v_cvt_pk_bf16_f32 v53, v51, v52\n\t"
    "v_accvgpr_write_b32 a77, v53\n\t"
    "v_accvgpr_read_b32 v56, a78\n\t"
    "v_lshlrev_b32 v54, 16, v56\n\t"
    "v_and_b32 v55, 0xffff0000, v56\n\t"
    "v_mul_f32 v54, s66, v54\n\t"
    "v_mul_f32 v55, s66, v55\n\t"
    "v_cvt_pk_bf16_f32 v56, v54, v55\n\t"
    "v_accvgpr_write_b32 a78, v56\n\t"
    "v_accvgpr_read_b32 v59, a79\n\t"
    "v_lshlrev_b32 v57, 16, v59\n\t"
    "v_and_b32 v58, 0xffff0000, v59\n\t"
    "v_mul_f32 v57, s66, v57\n\t"
    "v_mul_f32 v58, s66, v58\n\t"
    "v_cvt_pk_bf16_f32 v59, v57, v58\n\t"
    "v_accvgpr_write_b32 a79, v59\n\t"
    "s_waitcnt vmcnt(11)\n\t"
    "v_accvgpr_read_b32 v50, a80\n\t"
    "v_lshlrev_b32 v48, 16, v50\n\t"
    "v_and_b32 v49, 0xffff0000, v50\n\t"
    "v_mul_f32 v48, s66, v48\n\t"
    "v_mul_f32 v49, s66, v49\n\t"
    "v_cvt_pk_bf16_f32 v50, v48, v49\n\t"
    "v_accvgpr_write_b32 a80, v50\n\t"
    "v_accvgpr_read_b32 v53, a81\n\t"
    "v_lshlrev_b32 v51, 16, v53\n\t"
    "v_and_b32 v52, 0xffff0000, v53\n\t"
    "v_mul_f32 v51, s66, v51\n\t"
    "v_mul_f32 v52, s66, v52\n\t"
    "v_cvt_pk_bf16_f32 v53, v51, v52\n\t"
    "v_accvgpr_write_b32 a81, v53\n\t"
    "v_accvgpr_read_b32 v56, a82\n\t"
    "v_lshlrev_b32 v54, 16, v56\n\t"
    "v_and_b32 v55, 0xffff0000, v56\n\t"
    "v_mul_f32 v54, s66, v54\n\t"
    "v_mul_f32 v55, s66, v55\n\t"
    "v_cvt_pk_bf16_f32 v56, v54, v55\n\t"
    "v_accvgpr_write_b32 a82, v56\n\t"
    "v_accvgpr_read_b32 v59, a83\n\t"
    "v_lshlrev_b32 v57, 16, v59\n\t"
    "v_and_b32 v58, 0xffff0000, v59\n\t"
    "v_mul_f32 v57, s66, v57\n\t"
    "v_mul_f32 v58, s66, v58\n\t"
    "v_cvt_pk_bf16_f32 v59, v57, v58\n\t"
    "v_accvgpr_write_b32 a83, v59\n\t"
    "s_waitcnt vmcnt(10)\n\t"
    "v_accvgpr_read_b32 v50, a84\n\t"
    "v_lshlrev_b32 v48, 16, v50\n\t"
    "v_and_b32 v49, 0xffff0000, v50\n\t"
    "v_mul_f32 v48, s66, v48\n\t"
    "v_mul_f32 v49, s66, v49\n\t"
    "v_cvt_pk_bf16_f32 v50, v48, v49\n\t"
    "v_accvgpr_write_b32 a84, v50\n\t"
    "v_accvgpr_read_b32 v53, a85\n\t"
    "v_lshlrev_b32 v51, 16, v53\n\t"
    "v_and_b32 v52, 0xffff0000, v53\n\t"
    "v_mul_f32 v51, s66, v51\n\t"
    "v_mul_f32 v52, s66, v52\n\t"
    "v_cvt_pk_bf16_f32 v53, v51, v52\n\t"
    "v_accvgpr_write_b32 a85, v53\n\t"
    "v_accvgpr_read_b32 v56, a86\n\t"
    "v_lshlrev_b32 v54, 16, v56\n\t"
    "v_and_b32 v55, 0xffff0000, v56\n\t"
    "v_mul_f32 v54, s66, v54\n\t"
    "v_mul_f32 v55, s66, v55\n\t"
    "v_cvt_pk_bf16_f32 v56, v54, v55\n\t"
    "v_accvgpr_write_b32 a86, v56\n\t"
    "v_accvgpr_read_b32 v59, a87\n\t"
    "v_lshlrev_b32 v57, 16, v59\n\t"
    "v_and_b32 v58, 0xffff0000, v59\n\t"
    "v_mul_f32 v57, s66, v57\n\t"
    "v_mul_f32 v58, s66, v58\n\t"
    "v_cvt_pk_bf16_f32 v59, v57, v58\n\t"
    "v_accvgpr_write_b32 a87, v59\n\t"
    "s_waitcnt vmcnt(9)\n\t"
    "v_accvgpr_read_b32 v50, a88\n\t"
    "v_lshlrev_b32 v48, 16, v50\n\t"
    "v_and_b32 v49, 0xffff0000, v50\n\t"
    "v_mul_f32 v48, s66, v48\n\t"
    "v_mul_f32 v49, s66, v49\n\t"
    "v_cvt_pk_bf16_f32 v50, v48, v49\n\t"
    "v_accvgpr_write_b32 a88, v50\n\t"
    "v_accvgpr_read_b32 v53, a89\n\t"
    "v_lshlrev_b32 v51, 16, v53\n\t"
    "v_and_b32 v52, 0xffff0000, v53\n\t"
    "v_mul_f32 v51, s66, v51\n\t"
    "v_mul_f32 v52, s66, v52\n\t"
    "v_cvt_pk_bf16_f32 v53, v51, v52\n\t"
    "v_accvgpr_write_b32 a89, v53\n\t"
    "v_accvgpr_read_b32 v56, a90\n\t"
    "v_lshlrev_b32 v54, 16, v56\n\t"
    "v_and_b32 v55, 0xffff0000, v56\n\t"
    "v_mul_f32 v54, s66, v54\n\t"
    "v_mul_f32 v55, s66, v55\n\t"
    "v_cvt_pk_bf16_f32 v56, v54, v55\n\t"
    "v_accvgpr_write_b32 a90, v56\n\t"
    "v_accvgpr_read_b32 v59, a91\n\t"
    "v_lshlrev_b32 v57, 16, v59\n\t"
    "v_and_b32 v58, 0xffff0000, v59\n\t"
    "v_mul_f32 v57, s66, v57\n\t"
    "v_mul_f32 v58, s66, v58\n\t"
    "v_cvt_pk_bf16_f32 v59, v57, v58\n\t"
    "v_accvgpr_write_b32 a91, v59\n\t"
    "s_waitcnt vmcnt(8)\n\t"
    "v_accvgpr_read_b32 v50, a92\n\t"
    "v_lshlrev_b32 v48, 16, v50\n\t"
    "v_and_b32 v49, 0xffff0000, v50\n\t"
    "v_mul_f32 v48, s66, v48\n\t"
    "v_mul_f32 v49, s66, v49\n\t"
    "v_cvt_pk_bf16_f32 v50, v48, v49\n\t"
    "v_accvgpr_write_b32 a92, v50\n\t"
    "v_accvgpr_read_b32 v53, a93\n\t"
    "v_lshlrev_b32 v51, 16, v53\n\t"
    "v_and_b32 v52, 0xffff0000, v53\n\t"
    "v_mul_f32 v51, s66, v51\n\t"
    "v_mul_f32 v52, s66, v52\n\t"
    "v_cvt_pk_bf16_f32 v53, v51, v52\n\t"
    "v_accvgpr_write_b32 a93, v53\n\t"
    "v_accvgpr_read_b32 v56, a94\n\t"
    "v_lshlrev_b32 v54, 16, v56\n\t"
    "v_and_b32 v55, 0xffff0000, v56\n\t"
    "v_mul_f32 v54, s66, v54\n\t"
    "v_mul_f32 v55, s66, v55\n\t"
    "v_cvt_pk_bf16_f32 v56, v54, v55\n\t"
    "v_accvgpr_write_b32 a94, v56\n\t"
    "v_accvgpr_read_b32 v59, a95\n\t"
    "v_lshlrev_b32 v57, 16, v59\n\t"
    "v_and_b32 v58, 0xffff0000, v59\n\t"
    "v_mul_f32 v57, s66, v57\n\t"
    "v_mul_f32 v58, s66, v58\n\t"
    "v_cvt_pk_bf16_f32 v59, v57, v58\n\t"
    "v_accvgpr_write_b32 a95, v59\n\t"
    "ds_read_b128 v[0:3], v117 offset:0\n\t"
    "ds_read_b128 v[4:7], v117 offset:512\n\t"
    "ds_read_b128 v[8:11], v117 offset:1024\n\t"
    "ds_read_b128 v[12:15], v117 offset:1536\n\t"
    "ds_read_b128 v[16:19], v117 offset:4096\n\t"
    "ds_read_b128 v[20:23], v117 offset:4608\n\t"
    "ds_read_b128 v[24:27], v117 offset:5120\n\t"
    "ds_read_b128 v[28:31], v117 offset:5632\n\t"
    "s_waitcnt lgkmcnt(7)\n\t"
    "v_mfma_f32_16x16x32_bf16 v[32:35], a[64:67], v[0:3], 0\n\t"
    "s_waitcnt lgkmcnt(6)\n\t"
    "v_mfma_f32_16x16x32_bf16 v[32:35], a[68:71], v[4:7], v[32:35]\n\t"
    "s_waitcnt lgkmcnt(5)\n\t"
    "v_mfma_f32_16x16x32_bf16 v[32:35], a[72:75], v[8:11], v[32:35]\n\t"
    "s_waitcnt lgkmcnt(4)\n\t"
    "v_mfma_f32_16x16x32_bf16 v[32:35], a[76:79], v[12:15], v[32:35]\n\t"
    "v_mfma_f32_16x16x32_bf16 v[36:39], a[80:83], v[0:3], 0\n\t"
    "v_mfma_f32_16x16x32_bf16 v[36:39], a[84:87], v[4:7], v[36:39]\n\t"
    "v_mfma_f32_16x16x32_bf16 v[36:39], a[88:91], v[8:11], v[36:39]\n\t"
    "v_mfma_f32_16x16x32_bf16 v[36:39], a[92:95], v[12:15], v[36:39]\n\t"
    "ds_read_b128 v[0:3], v117 offset:8192\n\t"
    "ds_read_b128 v[4:7], v117 offset:8704\n\t"
    "ds_read_b128 v[8:11], v117 offset:9216\n\t"
    "ds_read_b128 v[12:15], v117 offset:9728\n\t"
    "s_waitcnt lgkmcnt(7)\n\t"
    "v_mfma_f32_16x16x32_bf16 v[40:43], a[64:67], v[16:19], 0\n\t"
    "v_exp_f32 v32, v32\n\t"
    "s_waitcnt lgkmcnt(6)\n\t"
    "v_mfma_f32_16x16x32_bf16 v[40:43], a[68:71], v[20:23], v[40:43]\n\t"
    "v_exp_f32 v33, v33\n\t"
    "s_waitcnt lgkmcnt(5)\n\t"
    "v_mfma_f32_16x16x32_bf16 v[40:43], a[72:75], v[24:27], v[40:43]\n\t"
    "v_exp_f32 v34, v34\n\t"
    "s_waitcnt lgkmcnt(4)\n\t"
    "v_mfma_f32_16x16x32_bf16 v[40:43], a[76:79], v[28:31], v[40:43]\n\t"
    "v_exp_f32 v35, v35\n\t"
    "v_mfma_f32_16x16x32_bf16 v[44:47], a[80:83], v[16:19], 0\n\t"
    "v_exp_f32 v36, v36\n\t"
    "v_mfma_f32_16x16x32_bf16 v[44:47], a[84:87], v[20:23], v[44:47]\n\t"
    "v_exp_f32 v37, v37\n\t"
    "v_mfma_f32_16x16x32_bf16 v[44:47], a[88:91], v[24:27], v[44:47]\n\t"
    "v_exp_f32 v38, v38\n\t"
    "v_mfma_f32_16x16x32_bf16 v[44:47], a[92:95], v[28:31], v[44:47]\n\t"
    "v_exp_f32 v39, v39\n\t"
    "v_cvt_pk_bf16_f32 v96, v32, v33\n\t"
    "v_cvt_pk_bf16_f32 v97, v34, v35\n\t"
    "v_cvt_pk_bf16_f32 v98, v36, v37\n\t"
    "v_cvt_pk_bf16_f32 v99, v38, v39\n\t"
    "ds_write_b128 v118, v[96:99] offset:0\n\t"
    "s_nop 0\n\t"
    "v_mfma_f32_16x16x32_bf16 v[64:67], v[104:107], v[96:99], v[64:67]\n\t"
    "ds_read_b128 v[16:19], v117 offset:12288\n\t"
    "ds_read_b128 v[20:23], v117 offset:12800\n\t"
    "ds_read_b128 v[24:27], v117 offset:13312\n\t"
    "ds_read_b128 v[28:31], v117 offset:13824\n\t"
    "s_waitcnt lgkmcnt(8)\n\t"
    "v_mfma_f32_16x16x32_bf16 v[32:35], a[64:67], v[0:3], 0\n\t"
    "v_exp_f32 v40, v40\n\t"
    "s_waitcnt lgkmcnt(7)\n\t"
    "v_mfma_f32_16x16x32_bf16 v[32:35], a[68:71], v[4:7], v[32:35]\n\t"
    "v_exp_f32 v41, v41\n\t"
    "s_waitcnt lgkmcnt(6)\n\t"
    "v_mfma_f32_16x16x32_bf16 v[32:35], a[72:75], v[8:11], v[32:35]\n\t"
    "v_exp_f32 v42, v42\n\t"
    "s_waitcnt lgkmcnt(5)\n\t"
    "v_mfma_f32_16x16x32_bf16 v[32:35], a[76:79], v[12:15], v[32:35]\n\t"
    "v_exp_f32 v43, v43\n\t"
    "v_mfma_f32_16x16x32_bf16 v[36:39], a[80:83], v[0:3], 0\n\t"
    "v_exp_f32 v44, v44\n\t"
    "v_mfma_f32_16x16x32_bf16 v[36:39], a[84:87], v[4:7], v[36:39]\n\t"
    "v_exp_f32 v45, v45\n\t"
    "v_mfma_f32_16x16x32_bf16 v[36:39], a[88:91], v[8:11], v[36:39]\n\t"
    "v_exp_f32 v46, v46\n\t"
    "v_mfma_f32_16x16x32_bf16 v[36:39], a[92:95], v[12:15], v[36:39]\n\t"
    "v_exp_f32 v47, v47\n\t"
    "v_cvt_pk_bf16_f32 v100, v40, v41\n\t"
    "v_cvt_pk_bf16_f32 v101, v42, v43\n\t"
    "v_cvt_pk_bf16_f32 v102, v44, v45\n\t"
    "v_cvt_pk_bf16_f32 v103, v46, v47\n\t"
    "ds_write_b128 v118, v[100:103] offset:1024\n\t"
    "s_nop 0\n\t"
    "v_mfma_f32_16x16x32_bf16 v[68:71], v[104:107], v[100:103], v[68:71]\n\t"
    "ds_read_b128 v[0:3], v117 offset:16384\n\t"
    "ds_read_b128 v[4:7], v117 offset:16896\n\t"
    "ds_read_b128 v[8:11], v117 offset:17408\n\t"
    "ds_read_b128 v[12:15], v117 offset:17920\n\t"
    "s_waitcnt lgkmcnt(8)\n\t"
    "v_mfma_f32_16x16x32_bf16 v[40:43], a[64:67], v[16:19], 0\n\t"
    "v_exp_f32 v32, v32\n\t"
    "s_waitcnt lgkmcnt(7)\n\t"
    "v_mfma_f32_16x16x32_bf16 v[40:43], a[68:71], v[20:23], v[40:43]\n\t"
    "v_exp_f32 v33, v33\n\t"
    "s_waitcnt lgkmcnt(6)\n\t"
    "v_mfma_f32_16x16x32_bf16 v[40:43], a[72:75], v[24:27], v[40:43]\n\t"
    "v_exp_f32 v34, v34\n\t"
    "s_waitcnt lgkmcnt(5)\n\t"
    "v_mfma_f32_16x16x32_bf16 v[40:43], a[76:79], v[28:31], v[40:43]\n\t"
    "v_exp_f32 v35, v35\n\t"
    "v_mfma_f32_16x16x32_bf16 v[44:47], a[80:83], v[16:19], 0\n\t"
    "v_exp_f32 v36, v36\n\t"
    "v_mfma_f32_16x16x32_bf16 v[44:47], a[84:87], v[20:23], v[44:47]\n\t"
    "v_exp_f32 v37, v37\n\t"
    "v_mfma_f32_16x16x32_bf16 v[44:47], a[88:91], v[24:27], v[44:47]\n\t"
    "v_exp_f32 v38, v38\n\t"
    "v_mfma_f32_16x16x32_bf16 v[44:47], a[92:95], v[28:31], v[44:47]\n\t"
    "v_exp_f32 v39, v39\n\t"
    "v_cvt_pk_bf16_f32 v96, v32, v33\n\t"
    "v_cvt_pk_bf16_f32 v97, v34, v35\n\t"
    "v_cvt_pk_bf16_f32 v98, v36, v37\n\t"
    "v_cvt_pk_bf16_f32 v99, v38, v39\n\t"
    "ds_write_b128 v118, v[96:99] offset:2048\n\t"
    "s_nop 0\n\t"
    "v_mfma_f32_16x16x32_bf16 v[72:75], v[104:107], v[96:99], v[72:75]\n\t"
    "ds_read_b128 v[16:19], v117 offset:20480\n\t"
    "ds_read_b128 v[20:23], v117 offset:20992\n\t"
    "ds_read_b128 v[24:27], v117 offset:21504\n\t"
    "ds_read_b128 v[28:31], v117 offset:22016\n\t"
    "s_waitcnt lgkmcnt(8)\n\t"
    "v_mfma_f32_16x16x32_bf16 v[32:35], a[64:67], v[0:3], 0\n\t"
    "v_exp_f32 v40, v40\n\t"
    "s_waitcnt lgkmcnt(7)\n\t"
    "v_mfma_f32_16x16x32_bf16 v[32:35], a[68:71], v[4:7], v[32:35]\n\t"
    "v_exp_f32 v41, v41\n\t"
    "s_waitcnt lgkmcnt(6)\n\t"
    "v_mfma_f32_16x16x32_bf16 v[32:35], a[72:75], v[8:11], v[32:35]\n\t"
    "v_exp_f32 v42, v42\n\t"
    "s_waitcnt lgkmcnt(5)\n\t"
    "v_mfma_f32_16x16x32_bf16 v[32:35], a[76:79], v[12:15], v[32:35]\n\t"
    "v_exp_f32 v43, v43\n\t"
    "v_mfma_f32_16x16x32_bf16 v[36:39], a[80:83], v[0:3], 0\n\t"
    "v_exp_f32 v44, v44\n\t"
    "v_mfma_f32_16x16x32_bf16 v[36:39], a[84:87], v[4:7], v[36:39]\n\t"
    "v_exp_f32 v45, v45\n\t"
    "v_mfma_f32_16x16x32_bf16 v[36:39], a[88:91], v[8:11], v[36:39]\n\t"
    "v_exp_f32 v46, v46\n\t"
    "v_mfma_f32_16x16x32_bf16 v[36:39], a[92:95], v[12:15], v[36:39]\n\t"
    "v_exp_f32 v47, v47\n\t"
    "v_cvt_pk_bf16_f32 v100, v40, v41\n\t"
    "v_cvt_pk_bf16_f32 v101, v42, v43\n\t"
    "v_cvt_pk_bf16_f32 v102, v44, v45\n\t"
    "v_cvt_pk_bf16_f32 v103, v46, v47\n\t"
    "ds_write_b128 v118, v[100:103] offset:3072\n\t"
    "s_nop 0\n\t"
    "v_mfma_f32_16x16x32_bf16 v[76:79], v[104:107], v[100:103], v[76:79]\n\t"
    "ds_read_b128 v[0:3], v117 offset:24576\n\t"
    "ds_read_b128 v[4:7], v117 offset:25088\n\t"
    "ds_read_b128 v[8:11], v117 offset:25600\n\t"
    "ds_read_b128 v[12:15], v117 offset:26112\n\t"
    "s_waitcnt lgkmcnt(8)\n\t"
    "v_mfma_f32_16x16x32_bf16 v[40:43], a[64:67], v[16:19], 0\n\t"
    "v_exp_f32 v32, v32\n\t"
    "s_waitcnt lgkmcnt(7)\n\t"
    "v_mfma_f32_16x16x32_bf16 v[40:43], a[68:71], v[20:23], v[40:43]\n\t"
    "v_exp_f32 v33, v33\n\t"
    "s_waitcnt lgkmcnt(6)\n\t"
    "v_mfma_f32_16x16x32_bf16 v[40:43], a[72:75], v[24:27], v[40:43]\n\t"
    "v_exp_f32 v34, v34\n\t"
    "s_waitcnt lgkmcnt(5)\n\t"
    "v_mfma_f32_16x16x32_bf16 v[40:43], a[76:79], v[28:31], v[40:43]\n\t"
    "v_exp_f32 v35, v35\n\t"
    "v_mfma_f32_16x16x32_bf16 v[44:47], a[80:83], v[16:19], 0\n\t"
    "v_exp_f32 v36, v36\n\t"
    "v_mfma_f32_16x16x32_bf16 v[44:47], a[84:87], v[20:23], v[44:47]\n\t"
    "v_exp_f32 v37, v37\n\t"
    "v_mfma_f32_16x16x32_bf16 v[44:47], a[88:91], v[24:27], v[44:47]\n\t"
    "v_exp_f32 v38, v38\n\t"
    "v_mfma_f32_16x16x32_bf16 v[44:47], a[92:95], v[28:31], v[44:47]\n\t"
    "v_exp_f32 v39, v39\n\t"
    "v_cvt_pk_bf16_f32 v96, v32, v33\n\t"
    "v_cvt_pk_bf16_f32 v97, v34, v35\n\t"
    "v_cvt_pk_bf16_f32 v98, v36, v37\n\t"
    "v_cvt_pk_bf16_f32 v99, v38, v39\n\t"
    "ds_write_b128 v118, v[96:99] offset:4096\n\t"
    "s_nop 0\n\t"
    "v_mfma_f32_16x16x32_bf16 v[80:83], v[104:107], v[96:99], v[80:83]\n\t"
    "ds_read_b128 v[16:19], v117 offset:28672\n\t"
    "ds_read_b128 v[20:23], v117 offset:29184\n\t"
    "ds_read_b128 v[24:27], v117 offset:29696\n\t"
    "ds_read_b128 v[28:31], v117 offset:30208\n\t"
    "s_waitcnt lgkmcnt(8)\n\t"
    "v_mfma_f32_16x16x32_bf16 v[32:35], a[64:67], v[0:3], 0\n\t"
    "v_exp_f32 v40, v40\n\t"
    "s_waitcnt lgkmcnt(7)\n\t"
    "v_mfma_f32_16x16x32_bf16 v[32:35], a[68:71], v[4:7], v[32:35]\n\t"
    "v_exp_f32 v41, v41\n\t"
    "s_waitcnt lgkmcnt(6)\n\t"
    "v_mfma_f32_16x16x32_bf16 v[32:35], a[72:75], v[8:11], v[32:35]\n\t"
    "v_exp_f32 v42, v42\n\t"
    "s_waitcnt lgkmcnt(5)\n\t"
    "v_mfma_f32_16x16x32_bf16 v[32:35], a[76:79], v[12:15], v[32:35]\n\t"
    "v_exp_f32 v43, v43\n\t"
    "v_mfma_f32_16x16x32_bf16 v[36:39], a[80:83], v[0:3], 0\n\t"
    "v_exp_f32 v44, v44\n\t"
    "v_mfma_f32_16x16x32_bf16 v[36:39], a[84:87], v[4:7], v[36:39]\n\t"
    "v_exp_f32 v45, v45\n\t"
    "v_mfma_f32_16x16x32_bf16 v[36:39], a[88:91], v[8:11], v[36:39]\n\t"
    "v_exp_f32 v46, v46\n\t"
    "v_mfma_f32_16x16x32_bf16 v[36:39], a[92:95], v[12:15], v[36:39]\n\t"
    "v_exp_f32 v47, v47\n\t"
    "v_cvt_pk_bf16_f32 v100, v40, v41\n\t"
    "v_cvt_pk_bf16_f32 v101, v42, v43\n\t"
    "v_cvt_pk_bf16_f32 v102, v44, v45\n\t"
    "v_cvt_pk_bf16_f32 v103, v46, v47\n\t"
    "ds_write_b128 v118, v[100:103] offset:5120\n\t"
    "s_nop 0\n\t"
    "v_mfma_f32_16x16x32_bf16 v[84:87], v[104:107], v[100:103], v[84:87]\n\t"
    "s_waitcnt lgkmcnt(4)\n\t"
    "v_mfma_f32_16x16x32_bf16 v[40:43], a[64:67], v[16:19], 0\n\t"
    "v_exp_f32 v32, v32\n\t"
    "s_waitcnt lgkmcnt(3)\n\t"
    "v_mfma_f32_16x16x32_bf16 v[40:43], a[68:71], v[20:23], v[40:43]\n\t"
    "v_exp_f32 v33, v33\n\t"
    "s_waitcnt lgkmcnt(2)\n\t"
    "v_mfma_f32_16x16x32_bf16 v[40:43], a[72:75], v[24:27], v[40:43]\n\t"
    "v_exp_f32 v34, v34\n\t"
    "s_waitcnt lgkmcnt(1)\n\t"
    "v_mfma_f32_16x16x32_bf16 v[40:43], a[76:79], v[28:31], v[40:43]\n\t"
    "v_exp_f32 v35, v35\n\t"
    "v_mfma_f32_16x16x32_bf16 v[44:47], a[80:83], v[16:19], 0\n\t"
    "v_exp_f32 v36, v36\n\t"
    "v_mfma_f32_16x16x32_bf16 v[44:47], a[84:87], v[20:23], v[44:47]\n\t"
    "v_exp_f32 v37, v37\n\t"
    "v_mfma_f32_16x16x32_bf16 v[44:47], a[88:91], v[24:27], v[44:47]\n\t"
    "v_exp_f32 v38, v38\n\t"
    "v_mfma_f32_16x16x32_bf16 v[44:47], a[92:95], v[28:31], v[44:47]\n\t"
    "v_exp_f32 v39, v39\n\t"
    "v_cvt_pk_bf16_f32 v96, v32, v33\n\t"
    "v_cvt_pk_bf16_f32 v97, v34, v35\n\t"
    "v_cvt_pk_bf16_f32 v98, v36, v37\n\t"
    "v_cvt_pk_bf16_f32 v99, v38, v39\n\t"
    "ds_write_b128 v118, v[96:99] offset:6144\n\t"
    "s_nop 0\n\t"
    "v_mfma_f32_16x16x32_bf16 v[88:91], v[104:107], v[96:99], v[88:91]\n\t"
    "v_exp_f32 v40, v40\n\t"
    "v_exp_f32 v41, v41\n\t"
    "v_exp_f32 v42, v42\n\t"
    "v_exp_f32 v43, v43\n\t"
    "v_exp_f32 v44, v44\n\t"
    "v_exp_f32 v45, v45\n\t"
    "v_exp_f32 v46, v46\n\t"
    "v_exp_f32 v47, v47\n\t"
    "v_cvt_pk_bf16_f32 v100, v40, v41\n\t"
    "v_cvt_pk_bf16_f32 v101, v42, v43\n\t"
    "v_cvt_pk_bf16_f32 v102, v44, v45\n\t"
    "v_cvt_pk_bf16_f32 v103, v46, v47\n\t"
    "ds_write_b128 v118, v[100:103] offset:7168\n\t"
    "s_nop 0\n\t"
    "v_mfma_f32_16x16x32_bf16 v[92:95], v[104:107], v[100:103], v[92:95]\n\t"
    "s_waitcnt lgkmcnt(0)\n\t"
    "s_barrier\n\t"
    "s_waitcnt vmcnt(7)\n\t"
    "v_accvgpr_read_b32 v50, a96\n\t"
    "v_lshlrev_b32 v48, 16, v50\n\t"
    "v_and_b32 v49, 0xffff0000, v50\n\t"
    "v_mul_f32 v48, s66, v48\n\t"
    "v_mul_f32 v49, s66, v49\n\t"
    "v_cvt_pk_bf16_f32 v50, v48, v49\n\t"
    "v_accvgpr_write_b32 a96, v50\n\t"
    "v_accvgpr_read_b32 v53, a97\n\t"
    "v_lshlrev_b32 v51, 16, v53\n\t"
    "v_and_b32 v52, 0xffff0000, v53\n\t"
    "v_mul_f32 v51, s66, v51\n\t"
    "v_mul_f32 v52, s66, v52\n\t"
    "v_cvt_pk_bf16_f32 v53, v51, v52\n\t"
    "v_accvgpr_write_b32 a97, v53\n\t"
    "v_accvgpr_read_b32 v56, a98\n\t"
    "v_lshlrev_b32 v54, 16, v56\n\t"
    "v_and_b32 v55, 0xffff0000, v56\n\t"
    "v_mul_f32 v54, s66, v54\n\t"
    "v_mul_f32 v55, s66, v55\n\t"
    "v_cvt_pk_bf16_f32 v56, v54, v55\n\t"
    "v_accvgpr_write_b32 a98, v56\n\t"
    "v_accvgpr_read_b32 v59, a99\n\t"
    "v_lshlrev_b32 v57, 16, v59\n\t"
    "v_and_b32 v58, 0xffff0000, v59\n\t"
    "v_mul_f32 v57, s66, v57\n\t"
    "v_mul_f32 v58, s66, v58\n\t"
    "v_cvt_pk_bf16_f32 v59, v57, v58\n\t"
    "v_accvgpr_write_b32 a99, v59\n\t"
    "s_waitcnt vmcnt(6)\n\t"
    "v_accvgpr_read_b32 v50, a100\n\t"
    "v_lshlrev_b32 v48, 16, v50\n\t"
    "v_and_b32 v49, 0xffff0000, v50\n\t"
    "v_mul_f32 v48, s66, v48\n\t"
    "v_mul_f32 v49, s66, v49\n\t"
    "v_cvt_pk_bf16_f32 v50, v48, v49\n\t"
    "v_accvgpr_write_b32 a100, v50\n\t"
    "v_accvgpr_read_b32 v53, a101\n\t"
    "v_lshlrev_b32 v51, 16, v53\n\t"
    "v_and_b32 v52, 0xffff0000, v53\n\t"
    "v_mul_f32 v51, s66, v51\n\t"
    "v_mul_f32 v52, s66, v52\n\t"
    "v_cvt_pk_bf16_f32 v53, v51, v52\n\t"
    "v_accvgpr_write_b32 a101, v53\n\t"
    "v_accvgpr_read_b32 v56, a102\n\t"
    "v_lshlrev_b32 v54, 16, v56\n\t"
    "v_and_b32 v55, 0xffff0000, v56\n\t"
    "v_mul_f32 v54, s66, v54\n\t"
    "v_mul_f32 v55, s66, v55\n\t"
    "v_cvt_pk_bf16_f32 v56, v54, v55\n\t"
    "v_accvgpr_write_b32 a102, v56\n\t"
    "v_accvgpr_read_b32 v59, a103\n\t"
    "v_lshlrev_b32 v57, 16, v59\n\t"
    "v_and_b32 v58, 0xffff0000, v59\n\t"
    "v_mul_f32 v57, s66, v57\n\t"
    "v_mul_f32 v58, s66, v58\n\t"
    "v_cvt_pk_bf16_f32 v59, v57, v58\n\t"
    "v_accvgpr_write_b32 a103, v59\n\t"
    "s_waitcnt vmcnt(5)\n\t"
    "v_accvgpr_read_b32 v50, a104\n\t"
    "v_lshlrev_b32 v48, 16, v50\n\t"
    "v_and_b32 v49, 0xffff0000, v50\n\t"
    "v_mul_f32 v48, s66, v48\n\t"
    "v_mul_f32 v49, s66, v49\n\t"
    "v_cvt_pk_bf16_f32 v50, v48, v49\n\t"
    "v_accvgpr_write_b32 a104, v50\n\t"
    "v_accvgpr_read_b32 v53, a105\n\t"
    "v_lshlrev_b32 v51, 16, v53\n\t"
    "v_and_b32 v52, 0xffff0000, v53\n\t"
    "v_mul_f32 v51, s66, v51\n\t"
    "v_mul_f32 v52, s66, v52\n\t"
    "v_cvt_pk_bf16_f32 v53, v51, v52\n\t"
    "v_accvgpr_write_b32 a105, v53\n\t"
    "v_accvgpr_read_b32 v56, a106\n\t"
    "v_lshlrev_b32 v54, 16, v56\n\t"
    "v_and_b32 v55, 0xffff0000, v56\n\t"
    "v_mul_f32 v54, s66, v54\n\t"
    "v_mul_f32 v55, s66, v55\n\t"
    "v_cvt_pk_bf16_f32 v56, v54, v55\n\t"
    "v_accvgpr_write_b32 a106, v56\n\t"
    "v_accvgpr_read_b32 v59, a107\n\t"
    "v_lshlrev_b32 v57, 16, v59\n\t"
    "v_and_b32 v58, 0xffff0000, v59\n\t"
    "v_mul_f32 v57, s66, v57\n\t"
    "v_mul_f32 v58, s66, v58\n\t"
    "v_cvt_pk_bf16_f32 v59, v57, v58\n\t"
    "v_accvgpr_write_b32 a107, v59\n\t"
    "s_waitcnt vmcnt(4)\n\t"
    "v_accvgpr_read_b32 v50, a108\n\t"
    "v_lshlrev_b32 v48, 16, v50\n\t"
    "v_and_b32 v49, 0xffff0000, v50\n\t"
    "v_mul_f32 v48, s66, v48\n\t"
    "v_mul_f32 v49, s66, v49\n\t"
    "v_cvt_pk_bf16_f32 v50, v48, v49\n\t"
    "v_accvgpr_write_b32 a108, v50\n\t"
    "v_accvgpr_read_b32 v53, a109\n\t"
    "v_lshlrev_b32 v51, 16, v53\n\t"
    "v_and_b32 v52, 0xffff0000, v53\n\t"
    "v_mul_f32 v51, s66, v51\n\t"
    "v_mul_f32 v52, s66, v52\n\t"
    "v_cvt_pk_bf16_f32 v53, v51, v52\n\t"
    "v_accvgpr_write_b32 a109, v53\n\t"
    "v_accvgpr_read_b32 v56, a110\n\t"
    "v_lshlrev_b32 v54, 16, v56\n\t"
    "v_and_b32 v55, 0xffff0000, v56\n\t"
    "v_mul_f32 v54, s66, v54\n\t"
    "v_mul_f32 v55, s66, v55\n\t"
    "v_cvt_pk_bf16_f32 v56, v54, v55\n\t"
    "v_accvgpr_write_b32 a110, v56\n\t"
    "v_accvgpr_read_b32 v59, a111\n\t"
    "v_lshlrev_b32 v57, 16, v59\n\t"
    "v_and_b32 v58, 0xffff0000, v59\n\t"
    "v_mul_f32 v57, s66, v57\n\t"
    "v_mul_f32 v58, s66, v58\n\t"
    "v_cvt_pk_bf16_f32 v59, v57, v58\n\t"
    "v_accvgpr_write_b32 a111, v59\n\t"
    "s_waitcnt vmcnt(3)\n\t"
    "v_accvgpr_read_b32 v50, a112\n\t"
    "v_lshlrev_b32 v48, 16, v50\n\t"
    "v_and_b32 v49, 0xffff0000, v50\n\t"
    "v_mul_f32 v48, s66, v48\n\t"
    "v_mul_f32 v49, s66, v49\n\t"
    "v_cvt_pk_bf16_f32 v50, v48, v49\n\t"
    "v_accvgpr_write_b32 a112, v50\n\t"
    "v_accvgpr_read_b32 v53, a113\n\t"
    "v_lshlrev_b32 v51, 16, v53\n\t"
    "v_and_b32 v52, 0xffff0000, v53\n\t"
    "v_mul_f32 v51, s66, v51\n\t"
    "v_mul_f32 v52, s66, v52\n\t"
    "v_cvt_pk_bf16_f32 v53, v51, v52\n\t"
    "v_accvgpr_write_b32 a113, v53\n\t"
    "v_accvgpr_read_b32 v56, a114\n\t"
    "v_lshlrev_b32 v54, 16, v56\n\t"
    "v_and_b32 v55, 0xffff0000, v56\n\t"
    "v_mul_f32 v54, s66, v54\n\t"
    "v_mul_f32 v55, s66, v55\n\t"
    "v_cvt_pk_bf16_f32 v56, v54, v55\n\t"
    "v_accvgpr_write_b32 a114, v56\n\t"
    "v_accvgpr_read_b32 v59, a115\n\t"
    "v_lshlrev_b32 v57, 16, v59\n\t"
    "v_and_b32 v58, 0xffff0000, v59\n\t"
    "v_mul_f32 v57, s66, v57\n\t"
    "v_mul_f32 v58, s66, v58\n\t"
    "v_cvt_pk_bf16_f32 v59, v57, v58\n\t"
    "v_accvgpr_write_b32 a115, v59\n\t"
    "s_waitcnt vmcnt(2)\n\t"
    "v_accvgpr_read_b32 v50, a116\n\t"
    "v_lshlrev_b32 v48, 16, v50\n\t"
    "v_and_b32 v49, 0xffff0000, v50\n\t"
    "v_mul_f32 v48, s66, v48\n\t"
    "v_mul_f32 v49, s66, v49\n\t"
    "v_cvt_pk_bf16_f32 v50, v48, v49\n\t"
    "v_accvgpr_write_b32 a116, v50\n\t"
    "v_accvgpr_read_b32 v53, a117\n\t"
    "v_lshlrev_b32 v51, 16, v53\n\t"
    "v_and_b32 v52, 0xffff0000, v53\n\t"
    "v_mul_f32 v51, s66, v51\n\t"
    "v_mul_f32 v52, s66, v52\n\t"
    "v_cvt_pk_bf16_f32 v53, v51, v52\n\t"
    "v_accvgpr_write_b32 a117, v53\n\t"
    "v_accvgpr_read_b32 v56, a118\n\t"
    "v_lshlrev_b32 v54, 16, v56\n\t"
    "v_and_b32 v55, 0xffff0000, v56\n\t"
    "v_mul_f32 v54, s66, v54\n\t"
    "v_mul_f32 v55, s66, v55\n\t"
    "v_cvt_pk_bf16_f32 v56, v54, v55\n\t"
    "v_accvgpr_write_b32 a118, v56\n\t"
    "v_accvgpr_read_b32 v59, a119\n\t"
    "v_lshlrev_b32 v57, 16, v59\n\t"
    "v_and_b32 v58, 0xffff0000, v59\n\t"
    "v_mul_f32 v57, s66, v57\n\t"
    "v_mul_f32 v58, s66, v58\n\t"
    "v_cvt_pk_bf16_f32 v59, v57, v58\n\t"
    "v_accvgpr_write_b32 a119, v59\n\t"
    "s_waitcnt vmcnt(1)\n\t"
    "v_accvgpr_read_b32 v50, a120\n\t"
    "v_lshlrev_b32 v48, 16, v50\n\t"
    "v_and_b32 v49, 0xffff0000, v50\n\t"
    "v_mul_f32 v48, s66, v48\n\t"
    "v_mul_f32 v49, s66, v49\n\t"
    "v_cvt_pk_bf16_f32 v50, v48, v49\n\t"
    "v_accvgpr_write_b32 a120, v50\n\t"
    "v_accvgpr_read_b32 v53, a121\n\t"
    "v_lshlrev_b32 v51, 16, v53\n\t"
    "v_and_b32 v52, 0xffff0000, v53\n\t"
    "v_mul_f32 v51, s66, v51\n\t"
    "v_mul_f32 v52, s66, v52\n\t"
    "v_cvt_pk_bf16_f32 v53, v51, v52\n\t"
    "v_accvgpr_write_b32 a121, v53\n\t"
    "v_accvgpr_read_b32 v56, a122\n\t"
    "v_lshlrev_b32 v54, 16, v56\n\t"
    "v_and_b32 v55, 0xffff0000, v56\n\t"
    "v_mul_f32 v54, s66, v54\n\t"
    "v_mul_f32 v55, s66, v55\n\t"
    "v_cvt_pk_bf16_f32 v56, v54, v55\n\t"
    "v_accvgpr_write_b32 a122, v56\n\t"
    "v_accvgpr_read_b32 v59, a123\n\t"
    "v_lshlrev_b32 v57, 16, v59\n\t"
    "v_and_b32 v58, 0xffff0000, v59\n\t"
    "v_mul_f32 v57, s66, v57\n\t"
    "v_mul_f32 v58, s66, v58\n\t"
    "v_cvt_pk_bf16_f32 v59, v57, v58\n\t"
    "v_accvgpr_write_b32 a123, v59\n\t"
    "s_waitcnt vmcnt(0)\n\t"
    "v_accvgpr_read_b32 v50, a124\n\t"
    "v_lshlrev_b32 v48, 16, v50\n\t"
    "v_and_b32 v49, 0xffff0000, v50\n\t"
    "v_mul_f32 v48, s66, v48\n\t"
    "v_mul_f32 v49, s66, v49\n\t"
    "v_cvt_pk_bf16_f32 v50, v48, v49\n\t"
    "v_accvgpr_write_b32 a124, v50\n\t"
    "v_accvgpr_read_b32 v53, a125\n\t"
    "v_lshlrev_b32 v51, 16, v53\n\t"
    "v_and_b32 v52, 0xffff0000, v53\n\t"
    "v_mul_f32 v51, s66, v51\n\t"
    "v_mul_f32 v52, s66, v52\n\t"
    "v_cvt_pk_bf16_f32 v53, v51, v52\n\t"
    "v_accvgpr_write_b32 a125, v53\n\t"
    "v_accvgpr_read_b32 v56, a126\n\t"
    "v_lshlrev_b32 v54, 16, v56\n\t"
    "v_and_b32 v55, 0xffff0000, v56\n\t"
    "v_mul_f32 v54, s66, v54\n\t"
    "v_mul_f32 v55, s66, v55\n\t"
    "v_cvt_pk_bf16_f32 v56, v54, v55\n\t"
    "v_accvgpr_write_b32 a126, v56\n\t"
    "v_accvgpr_read_b32 v59, a127\n\t"
    "v_lshlrev_b32 v57, 16, v59\n\t"
    "v_and_b32 v58, 0xffff0000, v59\n\t"
    "v_mul_f32 v57, s66, v57\n\t"
    "v_mul_f32 v58, s66, v58\n\t"
    "v_cvt_pk_bf16_f32 v59, v57, v58\n\t"
    "v_accvgpr_write_b32 a127, v59\n\t"
    "ds_read_b128 v[0:3], v117 offset:0\n\t"
    "ds_read_b128 v[4:7], v117 offset:512\n\t"
    "ds_read_b128 v[8:11], v117 offset:1024\n\t"
    "ds_read_b128 v[12:15], v117 offset:1536\n\t"
    "ds_read_b128 v[16:19], v117 offset:4096\n\t"
    "ds_read_b128 v[20:23], v117 offset:4608\n\t"
    "ds_read_b128 v[24:27], v117 offset:5120\n\t"
    "ds_read_b128 v[28:31], v117 offset:5632\n\t"
    "s_waitcnt lgkmcnt(7)\n\t"
    "v_mfma_f32_16x16x32_bf16 v[32:35], a[96:99], v[0:3], 0\n\t"
    "s_waitcnt lgkmcnt(6)\n\t"
    "v_mfma_f32_16x16x32_bf16 v[32:35], a[100:103], v[4:7], v[32:35]\n\t"
    "s_waitcnt lgkmcnt(5)\n\t"
    "v_mfma_f32_16x16x32_bf16 v[32:35], a[104:107], v[8:11], v[32:35]\n\t"
    "s_waitcnt lgkmcnt(4)\n\t"
    "v_mfma_f32_16x16x32_bf16 v[32:35], a[108:111], v[12:15], v[32:35]\n\t"
    "v_mfma_f32_16x16x32_bf16 v[36:39], a[112:115], v[0:3], 0\n\t"
    "v_mfma_f32_16x16x32_bf16 v[36:39], a[116:119], v[4:7], v[36:39]\n\t"
    "v_mfma_f32_16x16x32_bf16 v[36:39], a[120:123], v[8:11], v[36:39]\n\t"
    "v_mfma_f32_16x16x32_bf16 v[36:39], a[124:127], v[12:15], v[36:39]\n\t"
    "ds_read_b128 v[0:3], v117 offset:8192\n\t"
    "ds_read_b128 v[4:7], v117 offset:8704\n\t"
    "ds_read_b128 v[8:11], v117 offset:9216\n\t"
    "ds_read_b128 v[12:15], v117 offset:9728\n\t"
    "s_waitcnt lgkmcnt(7)\n\t"
    "v_mfma_f32_16x16x32_bf16 v[40:43], a[96:99], v[16:19], 0\n\t"
    "v_exp_f32 v32, v32\n\t"
    "s_waitcnt lgkmcnt(6)\n\t"
    "v_mfma_f32_16x16x32_bf16 v[40:43], a[100:103], v[20:23], v[40:43]\n\t"
    "v_exp_f32 v33, v33\n\t"
    "s_waitcnt lgkmcnt(5)\n\t"
    "v_mfma_f32_16x16x32_bf16 v[40:43], a[104:107], v[24:27], v[40:43]\n\t"
    "v_exp_f32 v34, v34\n\t"
    "s_waitcnt lgkmcnt(4)\n\t"
    "v_mfma_f32_16x16x32_bf16 v[40:43], a[108:111], v[28:31], v[40:43]\n\t"
    "v_exp_f32 v35, v35\n\t"
    "v_mfma_f32_16x16x32_bf16 v[44:47], a[112:115], v[16:19], 0\n\t"
    "v_exp_f32 v36, v36\n\t"
    "v_mfma_f32_16x16x32_bf16 v[44:47], a[116:119], v[20:23], v[44:47]\n\t"
    "v_exp_f32 v37, v37\n\t"
    "v_mfma_f32_16x16x32_bf16 v[44:47], a[120:123], v[24:27], v[44:47]\n\t"
    "v_exp_f32 v38, v38\n\t"
    "v_mfma_f32_16x16x32_bf16 v[44:47], a[124:127], v[28:31], v[44:47]\n\t"
    "v_exp_f32 v39, v39\n\t"
    "v_cvt_pk_bf16_f32 v96, v32, v33\n\t"
    "v_cvt_pk_bf16_f32 v97, v34, v35\n\t"
    "v_cvt_pk_bf16_f32 v98, v36, v37\n\t"
    "v_cvt_pk_bf16_f32 v99, v38, v39\n\t"
    "ds_write_b128 v118, v[96:99] offset:32768\n\t"
    "s_nop 0\n\t"
    "v_mfma_f32_16x16x32_bf16 v[64:67], v[104:107], v[96:99], v[64:67]\n\t"
    "ds_read_b128 v[16:19], v117 offset:12288\n\t"
    "ds_read_b128 v[20:23], v117 offset:12800\n\t"
    "ds_read_b128 v[24:27], v117 offset:13312\n\t"
    "ds_read_b128 v[28:31], v117 offset:13824\n\t"
    "s_waitcnt lgkmcnt(8)\n\t"
    "v_mfma_f32_16x16x32_bf16 v[32:35], a[96:99], v[0:3], 0\n\t"
    "v_exp_f32 v40, v40\n\t"
    "s_waitcnt lgkmcnt(7)\n\t"
    "v_mfma_f32_16x16x32_bf16 v[32:35], a[100:103], v[4:7], v[32:35]\n\t"
    "v_exp_f32 v41, v41\n\t"
    "s_waitcnt lgkmcnt(6)\n\t"
    "v_mfma_f32_16x16x32_bf16 v[32:35], a[104:107], v[8:11], v[32:35]\n\t"
    "v_exp_f32 v42, v42\n\t"
    "s_waitcnt lgkmcnt(5)\n\t"
    "v_mfma_f32_16x16x32_bf16 v[32:35], a[108:111], v[12:15], v[32:35]\n\t"
    "v_exp_f32 v43, v43\n\t"
    "v_mfma_f32_16x16x32_bf16 v[36:39], a[112:115], v[0:3], 0\n\t"
    "v_exp_f32 v44, v44\n\t"
    "v_mfma_f32_16x16x32_bf16 v[36:39], a[116:119], v[4:7], v[36:39]\n\t"
    "v_exp_f32 v45, v45\n\t"
    "v_mfma_f32_16x16x32_bf16 v[36:39], a[120:123], v[8:11], v[36:39]\n\t"
    "v_exp_f32 v46, v46\n\t"
    "v_mfma_f32_16x16x32_bf16 v[36:39], a[124:127], v[12:15], v[36:39]\n\t"
    "v_exp_f32 v47, v47\n\t"
    "v_cvt_pk_bf16_f32 v100, v40, v41\n\t"
    "v_cvt_pk_bf16_f32 v101, v42, v43\n\t"
    "v_cvt_pk_bf16_f32 v102, v44, v45\n\t"
    "v_cvt_pk_bf16_f32 v103, v46, v47\n\t"
    "ds_write_b128 v118, v[100:103] offset:33792\n\t"
    "s_nop 0\n\t"
    "v_mfma_f32_16x16x32_bf16 v[68:71], v[104:107], v[100:103], v[68:71]\n\t"
    "ds_read_b128 v[0:3], v117 offset:16384\n\t"
    "ds_read_b128 v[4:7], v117 offset:16896\n\t"
    "ds_read_b128 v[8:11], v117 offset:17408\n\t"
    "ds_read_b128 v[12:15], v117 offset:17920\n\t"
    "s_waitcnt lgkmcnt(8)\n\t"
    "v_mfma_f32_16x16x32_bf16 v[40:43], a[96:99], v[16:19], 0\n\t"
    "v_exp_f32 v32, v32\n\t"
    "s_waitcnt lgkmcnt(7)\n\t"
    "v_mfma_f32_16x16x32_bf16 v[40:43], a[100:103], v[20:23], v[40:43]\n\t"
    "v_exp_f32 v33, v33\n\t"
    "s_waitcnt lgkmcnt(6)\n\t"
    "v_mfma_f32_16x16x32_bf16 v[40:43], a[104:107], v[24:27], v[40:43]\n\t"
    "v_exp_f32 v34, v34\n\t"
    "s_waitcnt lgkmcnt(5)\n\t"
    "v_mfma_f32_16x16x32_bf16 v[40:43], a[108:111], v[28:31], v[40:43]\n\t"
    "v_exp_f32 v35, v35\n\t"
    "v_mfma_f32_16x16x32_bf16 v[44:47], a[112:115], v[16:19], 0\n\t"
    "v_exp_f32 v36, v36\n\t"
    "v_mfma_f32_16x16x32_bf16 v[44:47], a[116:119], v[20:23], v[44:47]\n\t"
    "v_exp_f32 v37, v37\n\t"
    "v_mfma_f32_16x16x32_bf16 v[44:47], a[120:123], v[24:27], v[44:47]\n\t"
    "v_exp_f32 v38, v38\n\t"
    "v_mfma_f32_16x16x32_bf16 v[44:47], a[124:127], v[28:31], v[44:47]\n\t"
    "v_exp_f32 v39, v39\n\t"
    "v_cvt_pk_bf16_f32 v96, v32, v33\n\t"
    "v_cvt_pk_bf16_f32 v97, v34, v35\n\t"
    "v_cvt_pk_bf16_f32 v98, v36, v37\n\t"
    "v_cvt_pk_bf16_f32 v99, v38, v39\n\t"
    "ds_write_b128 v118, v[96:99] offset:34816\n\t"
    "s_nop 0\n\t"
    "v_mfma_f32_16x16x32_bf16 v[72:75], v[104:107], v[96:99], v[72:75]\n\t"
    "ds_read_b128 v[16:19], v117 offset:20480\n\t"
    "ds_read_b128 v[20:23], v117 offset:20992\n\t"
    "ds_read_b128 v[24:27], v117 offset:21504\n\t"
    "ds_read_b128 v[28:31], v117 offset:22016\n\t"
    "s_waitcnt lgkmcnt(8)\n\t"
    "v_mfma_f32_16x16x32_bf16 v[32:35], a[96:99], v[0:3], 0\n\t"
    "v_exp_f32 v40, v40\n\t"
    "s_waitcnt lgkmcnt(7)\n\t"
    "v_mfma_f32_16x16x32_bf16 v[32:35], a[100:103], v[4:7], v[32:35]\n\t"
    "v_exp_f32 v41, v41\n\t"
    "s_waitcnt lgkmcnt(6)\n\t"
    "v_mfma_f32_16x16x32_bf16 v[32:35], a[104:107], v[8:11], v[32:35]\n\t"
    "v_exp_f32 v42, v42\n\t"
    "s_waitcnt lgkmcnt(5)\n\t"
    "v_mfma_f32_16x16x32_bf16 v[32:35], a[108:111], v[12:15], v[32:35]\n\t"
    "v_exp_f32 v43, v43\n\t"
    "v_mfma_f32_16x16x32_bf16 v[36:39], a[112:115], v[0:3], 0\n\t"
    "v_exp_f32 v44, v44\n\t"
    "v_mfma_f32_16x16x32_bf16 v[36:39], a[116:119], v[4:7], v[36:39]\n\t"
    "v_exp_f32 v45, v45\n\t"
    "v_mfma_f32_16x16x32_bf16 v[36:39], a[120:123], v[8:11], v[36:39]\n\t"
    "v_exp_f32 v46, v46\n\t"
    "v_mfma_f32_16x16x32_bf16 v[36:39], a[124:127], v[12:15], v[36:39]\n\t"
    "v_exp_f32 v47, v47\n\t"
    "v_cvt_pk_bf16_f32 v100, v40, v41\n\t"
    "v_cvt_pk_bf16_f32 v101, v42, v43\n\t"
    "v_cvt_pk_bf16_f32 v102, v44, v45\n\t"
    "v_cvt_pk_bf16_f32 v103, v46, v47\n\t"
    "ds_write_b128 v118, v[100:103] offset:35840\n\t"
    "s_nop 0\n\t"
    "v_mfma_f32_16x16x32_bf16 v[76:79], v[104:107], v[100:103], v[76:79]\n\t"
    "ds_read_b128 v[0:3], v117 offset:24576\n\t"
    "ds_read_b128 v[4:7], v117 offset:25088\n\t"
    "ds_read_b128 v[8:11], v117 offset:25600\n\t"
    "ds_read_b128 v[12:15], v117 offset:26112\n\t"
    "s_waitcnt lgkmcnt(8)\n\t"
    "v_mfma_f32_16x16x32_bf16 v[40:43], a[96:99], v[16:19], 0\n\t"
    "v_exp_f32 v32, v32\n\t"
    "s_waitcnt lgkmcnt(7)\n\t"
    "v_mfma_f32_16x16x32_bf16 v[40:43], a[100:103], v[20:23], v[40:43]\n\t"
    "v_exp_f32 v33, v33\n\t"
    "s_waitcnt lgkmcnt(6)\n\t"
    "v_mfma_f32_16x16x32_bf16 v[40:43], a[104:107], v[24:27], v[40:43]\n\t"
    "v_exp_f32 v34, v34\n\t"
    "s_waitcnt lgkmcnt(5)\n\t"
    "v_mfma_f32_16x16x32_bf16 v[40:43], a[108:111], v[28:31], v[40:43]\n\t"
    "v_exp_f32 v35, v35\n\t"
    "v_mfma_f32_16x16x32_bf16 v[44:47], a[112:115], v[16:19], 0\n\t"
    "v_exp_f32 v36, v36\n\t"
    "v_mfma_f32_16x16x32_bf16 v[44:47], a[116:119], v[20:23], v[44:47]\n\t"
    "v_exp_f32 v37, v37\n\t"
    "v_mfma_f32_16x16x32_bf16 v[44:47], a[120:123], v[24:27], v[44:47]\n\t"
    "v_exp_f32 v38, v38\n\t"
    "v_mfma_f32_16x16x32_bf16 v[44:47], a[124:127], v[28:31], v[44:47]\n\t"
    "v_exp_f32 v39, v39\n\t"
    "v_cvt_pk_bf16_f32 v96, v32, v33\n\t"
    "v_cvt_pk_bf16_f32 v97, v34, v35\n\t"
    "v_cvt_pk_bf16_f32 v98, v36, v37\n\t"
    "v_cvt_pk_bf16_f32 v99, v38, v39\n\t"
    "ds_write_b128 v118, v[96:99] offset:36864\n\t"
    "s_nop 0\n\t"
    "v_mfma_f32_16x16x32_bf16 v[80:83], v[104:107], v[96:99], v[80:83]\n\t"
    "ds_read_b128 v[16:19], v117 offset:28672\n\t"
    "ds_read_b128 v[20:23], v117 offset:29184\n\t"
    "ds_read_b128 v[24:27], v117 offset:29696\n\t"
    "ds_read_b128 v[28:31], v117 offset:30208\n\t"
    "s_waitcnt lgkmcnt(8)\n\t"
    "v_mfma_f32_16x16x32_bf16 v[32:35], a[96:99], v[0:3], 0\n\t"
    "v_exp_f32 v40, v40\n\t"
    "s_waitcnt lgkmcnt(7)\n\t"
    "v_mfma_f32_16x16x32_bf16 v[32:35], a[100:103], v[4:7], v[32:35]\n\t"
    "v_exp_f32 v41, v41\n\t"
    "s_waitcnt lgkmcnt(6)\n\t"
    "v_mfma_f32_16x16x32_bf16 v[32:35], a[104:107], v[8:11], v[32:35]\n\t"
    "v_exp_f32 v42, v42\n\t"
    "s_waitcnt lgkmcnt(5)\n\t"
    "v_mfma_f32_16x16x32_bf16 v[32:35], a[108:111], v[12:15], v[32:35]\n\t"
    "v_exp_f32 v43, v43\n\t"
    "v_mfma_f32_16x16x32_bf16 v[36:39], a[112:115], v[0:3], 0\n\t"
    "v_exp_f32 v44, v44\n\t"
    "v_mfma_f32_16x16x32_bf16 v[36:39], a[116:119], v[4:7], v[36:39]\n\t"
    "v_exp_f32 v45, v45\n\t"
    "v_mfma_f32_16x16x32_bf16 v[36:39], a[120:123], v[8:11], v[36:39]\n\t"
    "v_exp_f32 v46, v46\n\t"
    "v_mfma_f32_16x16x32_bf16 v[36:39], a[124:127], v[12:15], v[36:39]\n\t"
    "v_exp_f32 v47, v47\n\t"
    "v_cvt_pk_bf16_f32 v100, v40, v41\n\t"
    "v_cvt_pk_bf16_f32 v101, v42, v43\n\t"
    "v_cvt_pk_bf16_f32 v102, v44, v45\n\t"
    "v_cvt_pk_bf16_f32 v103, v46, v47\n\t"
    "ds_write_b128 v118, v[100:103] offset:37888\n\t"
    "s_nop 0\n\t"
    "v_mfma_f32_16x16x32_bf16 v[84:87], v[104:107], v[100:103], v[84:87]\n\t"
    "s_waitcnt lgkmcnt(4)\n\t"
    "v_mfma_f32_16x16x32_bf16 v[40:43], a[96:99], v[16:19], 0\n\t"
    "v_exp_f32 v32, v32\n\t"
    "s_waitcnt lgkmcnt(3)\n\t"
    "v_mfma_f32_16x16x32_bf16 v[40:43], a[100:103], v[20:23], v[40:43]\n\t"
    "v_exp_f32 v33, v33\n\t"
    "s_waitcnt lgkmcnt(2)\n\t"
    "v_mfma_f32_16x16x32_bf16 v[40:43], a[104:107], v[24:27], v[40:43]\n\t"
    "v_exp_f32 v34, v34\n\t"
    "s_waitcnt lgkmcnt(1)\n\t"
    "v_mfma_f32_16x16x32_bf16 v[40:43], a[108:111], v[28:31], v[40:43]\n\t"
    "v_exp_f32 v35, v35\n\t"
    "v_mfma_f32_16x16x32_bf16 v[44:47], a[112:115], v[16:19], 0\n\t"
    "v_exp_f32 v36, v36\n\t"
    "v_mfma_f32_16x16x32_bf16 v[44:47], a[116:119], v[20:23], v[44:47]\n\t"
    "v_exp_f32 v37, v37\n\t"
    "v_mfma_f32_16x16x32_bf16 v[44:47], a[120:123], v[24:27], v[44:47]\n\t"
    "v_exp_f32 v38, v38\n\t"
    "v_mfma_f32_16x16x32_bf16 v[44:47], a[124:127], v[28:31], v[44:47]\n\t"
    "v_exp_f32 v39, v39\n\t"
    "v_cvt_pk_bf16_f32 v96, v32, v33\n\t"
    "v_cvt_pk_bf16_f32 v97, v34, v35\n\t"
    "v_cvt_pk_bf16_f32 v98, v36, v37\n\t"
    "v_cvt_pk_bf16_f32 v99, v38, v39\n\t"
    "ds_write_b128 v118, v[96:99] offset:38912\n\t"
    "s_nop 0\n\t"
    "v_mfma_f32_16x16x32_bf16 v[88:91], v[104:107], v[96:99], v[88:91]\n\t"
    "v_exp_f32 v40, v40\n\t"
    "v_exp_f32 v41, v41\n\t"
    "v_exp_f32 v42, v42\n\t"
    "v_exp_f32 v43, v43\n\t"
    "v_exp_f32 v44, v44\n\t"
    "v_exp_f32 v45, v45\n\t"
    "v_exp_f32 v46, v46\n\t"
    "v_exp_f32 v47, v47\n\t"
    "v_cvt_pk_bf16_f32 v100, v40, v41\n\t"
    "v_cvt_pk_bf16_f32 v101, v42, v43\n\t"
    "v_cvt_pk_bf16_f32 v102, v44, v45\n\t"
    "v_cvt_pk_bf16_f32 v103, v46, v47\n\t"
    "ds_write_b128 v118, v[100:103] offset:39936\n\t"
    "s_nop 0\n\t"
    "v_mfma_f32_16x16x32_bf16 v[92:95], v[104:107], v[100:103], v[92:95]\n\t"
    "v_permlane32_swap_b32 v64, v68\n\t"
    "ds_write_b32 v125, v64 offset:0\n\t"
    "v_permlane32_swap_b32 v72, v76\n\t"
    "ds_write_b32 v125, v72 offset:512\n\t"
    "v_permlane32_swap_b32 v80, v84\n\t"
    "ds_write_b32 v125, v80 offset:1024\n\t"
    "s_nop 1\n\t"
    "v_permlane32_swap_b32 v88, v92\n\t"
    "ds_write_b32 v125, v88 offset:1536\n\t"
    "s_waitcnt lgkmcnt(0)\n\t"
    "s_barrier\n\t"
    "s_mov_b32 s53, 0x8000\n\t"
    "v_add_u32 v116, s53, v117\n\t"
    "ds_read_b128 v[0:3], v116 offset:0\n\t"
    "ds_read_b128 v[4:7], v116 offset:512\n\t"
    "ds_read_b128 v[8:11], v116 offset:1024\n\t"
    "ds_read_b128 v[12:15], v116 offset:1536\n\t"
    "ds_read_b128 v[16:19], v116 offset:4096\n\t"
    "ds_read_b128 v[20:23], v116 offset:4608\n\t"
    "ds_read_b128 v[24:27], v116 offset:5120\n\t"
    "ds_read_b128 v[28:31], v116 offset:5632\n\t"
    "s_waitcnt lgkmcnt(7)\n\t"
    "v_mfma_f32_16x16x32_bf16 v[32:35], a[0:3], v[0:3], 0\n\t"
    "s_waitcnt lgkmcnt(6)\n\t"
    "v_mfma_f32_16x16x32_bf16 v[32:35], a[4:7], v[4:7], v[32:35]\n\t"
    "s_waitcnt lgkmcnt(5)\n\t"
    "v_mfma_f32_16x16x32_bf16 v[32:35], a[8:11], v[8:11], v[32:35]\n\t"
    "s_waitcnt lgkmcnt(4)\n\t"
    "v_mfma_f32_16x16x32_bf16 v[32:35], a[12:15], v[12:15], v[32:35]\n\t"
    "v_mfma_f32_16x16x32_bf16 v[36:39], a[16:19], v[0:3], 0\n\t"
    "v_mfma_f32_16x16x32_bf16 v[36:39], a[20:23], v[4:7], v[36:39]\n\t"
    "v_mfma_f32_16x16x32_bf16 v[36:39], a[24:27], v[8:11], v[36:39]\n\t"
    "v_mfma_f32_16x16x32_bf16 v[36:39], a[28:31], v[12:15], v[36:39]\n\t"
    "v_mfma_f32_16x16x32_bf16 v[40:43], a[32:35], v[0:3], 0\n\t"
    "v_mfma_f32_16x16x32_bf16 v[40:43], a[36:39], v[4:7], v[40:43]\n\t"
    "v_mfma_f32_16x16x32_bf16 v[40:43], a[40:43], v[8:11], v[40:43]\n\t"
    "v_mfma_f32_16x16x32_bf16 v[40:43], a[44:47], v[12:15], v[40:43]\n\t"
    "v_mfma_f32_16x16x32_bf16 v[44:47], a[48:51], v[0:3], 0\n\t"
    "v_mfma_f32_16x16x32_bf16 v[44:47], a[52:55], v[4:7], v[44:47]\n\t"
    "v_mfma_f32_16x16x32_bf16 v[44:47], a[56:59], v[8:11], v[44:47]\n\t"
    "v_mfma_f32_16x16x32_bf16 v[44:47], a[60:63], v[12:15], v[44:47]\n\t"
    "v_mfma_f32_16x16x32_bf16 v[48:51], a[64:67], v[0:3], 0\n\t"
    "v_exp_f32 v32, v32\n\t"
    "v_mfma_f32_16x16x32_bf16 v[48:51], a[68:71], v[4:7], v[48:51]\n\t"
    "v_exp_f32 v33, v33\n\t"
    "v_mfma_f32_16x16x32_bf16 v[48:51], a[72:75], v[8:11], v[48:51]\n\t"
    "v_exp_f32 v34, v34\n\t"
    "v_mfma_f32_16x16x32_bf16 v[48:51], a[76:79], v[12:15], v[48:51]\n\t"
    "v_exp_f32 v35, v35\n\t"
    "v_mfma_f32_16x16x32_bf16 v[52:55], a[80:83], v[0:3], 0\n\t"
    "v_exp_f32 v36, v36\n\t"
    "v_mfma_f32_16x16x32_bf16 v[52:55], a[84:87], v[4:7], v[52:55]\n\t"
    "v_exp_f32 v37, v37\n\t"
    "v_mfma_f32_16x16x32_bf16 v[52:55], a[88:91], v[8:11], v[52:55]\n\t"
    "v_exp_f32 v38, v38\n\t"
    "v_mfma_f32_16x16x32_bf16 v[52:55], a[92:95], v[12:15], v[52:55]\n\t"
    "v_exp_f32 v39, v39\n\t"
    "v_mfma_f32_16x16x32_bf16 v[56:59], a[96:99], v[0:3], 0\n\t"
    "v_exp_f32 v40, v40\n\t"
    "v_mfma_f32_16x16x32_bf16 v[56:59], a[100:103], v[4:7], v[56:59]\n\t"
    "v_exp_f32 v41, v41\n\t"
    "v_mfma_f32_16x16x32_bf16 v[56:59], a[104:107], v[8:11], v[56:59]\n\t"
    "v_exp_f32 v42, v42\n\t"
    "v_mfma_f32_16x16x32_bf16 v[56:59], a[108:111], v[12:15], v[56:59]\n\t"
    "v_exp_f32 v43, v43\n\t"
    "v_mfma_f32_16x16x32_bf16 v[60:63], a[112:115], v[0:3], 0\n\t"
    "v_exp_f32 v44, v44\n\t"
    "v_mfma_f32_16x16x32_bf16 v[60:63], a[116:119], v[4:7], v[60:63]\n\t"
    "v_exp_f32 v45, v45\n\t"
    "v_mfma_f32_16x16x32_bf16 v[60:63], a[120:123], v[8:11], v[60:63]\n\t"
    "v_exp_f32 v46, v46\n\t"
    "v_mfma_f32_16x16x32_bf16 v[60:63], a[124:127], v[12:15], v[60:63]\n\t"
    "v_exp_f32 v47, v47\n\t"
    "s_add_u32 s53, s53, 0x2000\n\t"
    "s_and_b32 s53, s53, 0xffff\n\t"
    "v_add_u32 v116, s53, v117\n\t"
    "v_cvt_pk_bf16_f32 v96, v32, v33\n\t"
    "v_cvt_pk_bf16_f32 v97, v34, v35\n\t"
    "v_cvt_pk_bf16_f32 v98, v36, v37\n\t"
    "v_cvt_pk_bf16_f32 v99, v38, v39\n\t"
    "ds_write_b128 v118, v[96:99] offset:0\n\t"
    "v_cvt_pk_bf16_f32 v100, v40, v41\n\t"
    "v_cvt_pk_bf16_f32 v101, v42, v43\n\t"
    "v_cvt_pk_bf16_f32 v102, v44, v45\n\t"
    "v_cvt_pk_bf16_f32 v103, v46, v47\n\t"
    "ds_write_b128 v118, v[100:103] offset:1024\n\t"
    "ds_read_b128 v[0:3], v116 offset:0\n\t"
    "ds_read_b128 v[4:7], v116 offset:512\n\t"
    "v_mfma_f32_16x16x32_bf16 v[108:111], v[104:107], v[96:99], 0\n\t"
    "v_mfma_f32_16x16x32_bf16 v[108:111], v[104:107], v[100:103], v[108:111]\n\t"
    "s_waitcnt lgkmcnt(7)\n\t"
    "v_mfma_f32_16x16x32_bf16 v[32:35], a[0:3], v[16:19], 0\n\t"
    "v_exp_f32 v48, v48\n\t"
    "s_waitcnt lgkmcnt(6)\n\t"
    "v_mfma_f32_16x16x32_bf16 v[32:35], a[4:7], v[20:23], v[32:35]\n\t"
    "v_exp_f32 v49, v49\n\t"
    "s_waitcnt lgkmcnt(5)\n\t"
    "v_mfma_f32_16x16x32_bf16 v[32:35], a[8:11], v[24:27], v[32:35]\n\t"
    "v_exp_f32 v50, v50\n\t"
    "s_waitcnt lgkmcnt(4)\n\t"
    "v_mfma_f32_16x16x32_bf16 v[32:35], a[12:15], v[28:31], v[32:35]\n\t"
    "v_exp_f32 v51, v51\n\t"
    "v_mfma_f32_16x16x32_bf16 v[36:39], a[16:19], v[16:19], 0\n\t"
    "v_exp_f32 v52, v52\n\t"
    "v_mfma_f32_16x16x32_bf16 v[36:39], a[20:23], v[20:23], v[36:39]\n\t"
    "v_exp_f32 v53, v53\n\t"
    "v_mfma_f32_16x16x32_bf16 v[36:39], a[24:27], v[24:27], v[36:39]\n\t"
    "v_exp_f32 v54, v54\n\t"
    "v_mfma_f32_16x16x32_bf16 v[36:39], a[28:31], v[28:31], v[36:39]\n\t"
    "v_exp_f32 v55, v55\n\t"
    "v_mfma_f32_16x16x32_bf16 v[40:43], a[32:35], v[16:19], 0\n\t"
    "v_exp_f32 v56, v56\n\t"
    "v_mfma_f32_16x16x32_bf16 v[40:43], a[36:39], v[20:23], v[40:43]\n\t"
    "v_exp_f32 v57, v57\n\t"
    "v_mfma_f32_16x16x32_bf16 v[40:43], a[40:43], v[24:27], v[40:43]\n\t"
    "v_exp_f32 v58, v58\n\t"
    "v_mfma_f32_16x16x32_bf16 v[40:43], a[44:47], v[28:31], v[40:43]\n\t"
    "v_exp_f32 v59, v59\n\t"
    "v_mfma_f32_16x16x32_bf16 v[44:47], a[48:51], v[16:19], 0\n\t"
    "v_exp_f32 v60, v60\n\t"
    "v_mfma_f32_16x16x32_bf16 v[44:47], a[52:55], v[20:23], v[44:47]\n\t"
    "v_exp_f32 v61, v61\n\t"
    "v_mfma_f32_16x16x32_bf16 v[44:47], a[56:59], v[24:27], v[44:47]\n\t"
    "v_exp_f32 v62, v62\n\t"
    "v_mfma_f32_16x16x32_bf16 v[44:47], a[60:63], v[28:31], v[44:47]\n\t"
    "v_exp_f32 v63, v63\n\t"
    "v_cvt_pk_bf16_f32 v96, v48, v49\n\t"
    "v_cvt_pk_bf16_f32 v97, v50, v51\n\t"
    "v_cvt_pk_bf16_f32 v98, v52, v53\n\t"
    "v_cvt_pk_bf16_f32 v99, v54, v55\n\t"
    "ds_write_b128 v118, v[96:99] offset:2048\n\t"
    "v_cvt_pk_bf16_f32 v100, v56, v57\n\t"
    "v_cvt_pk_bf16_f32 v101, v58, v59\n\t"
    "v_cvt_pk_bf16_f32 v102, v60, v61\n\t"
    "v_cvt_pk_bf16_f32 v103, v62, v63\n\t"
    "ds_write_b128 v118, v[100:103] offset:3072\n\t"
    "ds_read_b128 v[8:11], v116 offset:1024\n\t"
    "ds_read_b128 v[12:15], v116 offset:1536\n\t"
    "v_mfma_f32_16x16x32_bf16 v[108:111], v[104:107], v[96:99], v[108:111]\n\t"
    "v_mfma_f32_16x16x32_bf16 v[108:111], v[104:107], v[100:103], v[108:111]\n\t"
    "v_mfma_f32_16x16x32_bf16 v[48:51], a[64:67], v[16:19], 0\n\t"
    "v_exp_f32 v32, v32\n\t"
    "v_mfma_f32_16x16x32_bf16 v[48:51], a[68:71], v[20:23], v[48:51]\n\t"
    "v_exp_f32 v33, v33\n\t"
    "v_mfma_f32_16x16x32_bf16 v[48:51], a[72:75], v[24:27], v[48:51]\n\t"
    "v_exp_f32 v34, v34\n\t"
    "v_mfma_f32_16x16x32_bf16 v[48:51], a[76:79], v[28:31], v[48:51]\n\t"
    "v_exp_f32 v35, v35\n\t"
    "v_mfma_f32_16x16x32_bf16 v[52:55], a[80:83], v[16:19], 0\n\t"
    "v_exp_f32 v36, v36\n\t"
    "v_mfma_f32_16x16x32_bf16 v[52:55], a[84:87], v[20:23], v[52:55]\n\t"
    "v_exp_f32 v37, v37\n\t"
    "v_mfma_f32_16x16x32_bf16 v[52:55], a[88:91], v[24:27], v[52:55]\n\t"
    "v_exp_f32 v38, v38\n\t"
    "v_mfma_f32_16x16x32_bf16 v[52:55], a[92:95], v[28:31], v[52:55]\n\t"
    "v_exp_f32 v39, v39\n\t"
    "v_mfma_f32_16x16x32_bf16 v[56:59], a[96:99], v[16:19], 0\n\t"
    "v_exp_f32 v40, v40\n\t"
    "v_mfma_f32_16x16x32_bf16 v[56:59], a[100:103], v[20:23], v[56:59]\n\t"
    "v_exp_f32 v41, v41\n\t"
    "v_mfma_f32_16x16x32_bf16 v[56:59], a[104:107], v[24:27], v[56:59]\n\t"
    "v_exp_f32 v42, v42\n\t"
    "v_mfma_f32_16x16x32_bf16 v[56:59], a[108:111], v[28:31], v[56:59]\n\t"
    "v_exp_f32 v43, v43\n\t"
    "v_mfma_f32_16x16x32_bf16 v[60:63], a[112:115], v[16:19], 0\n\t"
    "v_exp_f32 v44, v44\n\t"
    "v_mfma_f32_16x16x32_bf16 v[60:63], a[116:119], v[20:23], v[60:63]\n\t"
    "v_exp_f32 v45, v45\n\t"
    "v_mfma_f32_16x16x32_bf16 v[60:63], a[120:123], v[24:27], v[60:63]\n\t"
    "v_exp_f32 v46, v46\n\t"
    "v_mfma_f32_16x16x32_bf16 v[60:63], a[124:127], v[28:31], v[60:63]\n\t"
    "v_exp_f32 v47, v47\n\t"
    "v_cvt_pk_bf16_f32 v96, v32, v33\n\t"
    "v_cvt_pk_bf16_f32 v97, v34, v35\n\t"
    "v_cvt_pk_bf16_f32 v98, v36, v37\n\t"
    "v_cvt_pk_bf16_f32 v99, v38, v39\n\t"
    "ds_write_b128 v118, v[96:99] offset:4096\n\t"
    "v_cvt_pk_bf16_f32 v100, v40, v41\n\t"
    "v_cvt_pk_bf16_f32 v101, v42, v43\n\t"
    "v_cvt_pk_bf16_f32 v102, v44, v45\n\t"
    "v_cvt_pk_bf16_f32 v103, v46, v47\n\t"
    "ds_write_b128 v118, v[100:103] offset:5120\n\t"
    "ds_read_b128 v[16:19], v116 offset:4096\n\t"
    "ds_read_b128 v[20:23], v116 offset:4608\n\t"
    "v_mfma_f32_16x16x32_bf16 v[112:115], v[104:107], v[96:99], 0\n\t"
    "v_mfma_f32_16x16x32_bf16 v[112:115], v[104:107], v[100:103], v[112:115]\n\t"
    "s_waitcnt lgkmcnt(9)\n\t"
    "v_mfma_f32_16x16x32_bf16 v[32:35], a[0:3], v[0:3], 0\n\t"
    "v_exp_f32 v48, v48\n\t"
    "s_waitcnt lgkmcnt(8)\n\t"
    "v_mfma_f32_16x16x32_bf16 v[32:35], a[4:7], v[4:7], v[32:35]\n\t"
    "v_exp_f32 v49, v49\n\t"
    "s_waitcnt lgkmcnt(5)\n\t"
    "v_mfma_f32_16x16x32_bf16 v[32:35], a[8:11], v[8:11], v[32:35]\n\t"
    "v_exp_f32 v50, v50\n\t"
    "s_waitcnt lgkmcnt(4)\n\t"
    "v_mfma_f32_16x16x32_bf16 v[32:35], a[12:15], v[12:15], v[32:35]\n\t"
    "v_exp_f32 v51, v51\n\t"
    "v_mfma_f32_16x16x32_bf16 v[36:39], a[16:19], v[0:3], 0\n\t"
    "v_exp_f32 v52, v52\n\t"
    "v_mfma_f32_16x16x32_bf16 v[36:39], a[20:23], v[4:7], v[36:39]\n\t"
    "v_exp_f32 v53, v53\n\t"
    "v_mfma_f32_16x16x32_bf16 v[36:39], a[24:27], v[8:11], v[36:39]\n\t"
    "v_exp_f32 v54, v54\n\t"
    "v_mfma_f32_16x16x32_bf16 v[36:39], a[28:31], v[12:15], v[36:39]\n\t"
    "v_exp_f32 v55, v55\n\t"
    "v_mfma_f32_16x16x32_bf16 v[40:43], a[32:35], v[0:3], 0\n\t"
    "v_exp_f32 v56, v56\n\t"
    "v_mfma_f32_16x16x32_bf16 v[40:43], a[36:39], v[4:7], v[40:43]\n\t"
    "v_exp_f32 v57, v57\n\t"
    "v_mfma_f32_16x16x32_bf16 v[40:43], a[40:43], v[8:11], v[40:43]\n\t"
    "v_exp_f32 v58, v58\n\t"
    "v_mfma_f32_16x16x32_bf16 v[40:43], a[44:47], v[12:15], v[40:43]\n\t"
    "v_exp_f32 v59, v59\n\t"
    "v_mfma_f32_16x16x32_bf16 v[44:47], a[48:51], v[0:3], 0\n\t"
    "v_exp_f32 v60, v60\n\t"
    "v_mfma_f32_16x16x32_bf16 v[44:47], a[52:55], v[4:7], v[44:47]\n\t"
    "v_exp_f32 v61, v61\n\t"
    "v_mfma_f32_16x16x32_bf16 v[44:47], a[56:59], v[8:11], v[44:47]\n\t"
    "v_exp_f32 v62, v62\n\t"
    "v_mfma_f32_16x16x32_bf16 v[44:47], a[60:63], v[12:15], v[44:47]\n\t"
    "v_exp_f32 v63, v63\n\t"
    "v_cvt_pk_bf16_f32 v96, v48, v49\n\t"
    "v_cvt_pk_bf16_f32 v97, v50, v51\n\t"
    "v_cvt_pk_bf16_f32 v98, v52, v53\n\t"
    "v_cvt_pk_bf16_f32 v99, v54, v55\n\t"
    "ds_write_b128 v118, v[96:99] offset:6144\n\t"
    "v_cvt_pk_bf16_f32 v100, v56, v57\n\t"
    "v_cvt_pk_bf16_f32 v101, v58, v59\n\t"
    "v_cvt_pk_bf16_f32 v102, v60, v61\n\t"
    "v_cvt_pk_bf16_f32 v103, v62, v63\n\t"
    "ds_write_b128 v118, v[100:103] offset:7168\n\t"
    "ds_read_b128 v[24:27], v116 offset:5120\n\t"
    "ds_read_b128 v[28:31], v116 offset:5632\n\t"
    "s_waitcnt lgkmcnt(0)\n\t"
    "s_mov_b32 s52, 20\n\t"
    "Lqk_%=:\n\t"
    "v_mfma_f32_16x16x32_bf16 v[112:115], v[104:107], v[96:99], v[112:115]\n\t"
    "v_mfma_f32_16x16x32_bf16 v[112:115], v[104:107], v[100:103], v[112:115]\n\t"
    "v_mfma_f32_16x16x32_bf16 v[48:51], a[64:67], v[0:3], 0\n\t"
    "v_exp_f32 v32, v32\n\t"
    "v_mfma_f32_16x16x32_bf16 v[48:51], a[68:71], v[4:7], v[48:51]\n\t"
    "v_exp_f32 v33, v33\n\t"
    "v_mfma_f32_16x16x32_bf16 v[48:51], a[72:75], v[8:11], v[48:51]\n\t"
    "v_exp_f32 v34, v34\n\t"
    "v_mfma_f32_16x16x32_bf16 v[48:51], a[76:79], v[12:15], v[48:51]\n\t"
    "v_exp_f32 v35, v35\n\t"
    "v_mfma_f32_16x16x32_bf16 v[52:55], a[80:83], v[0:3], 0\n\t"
    "v_exp_f32 v36, v36\n\t"
    "v_mfma_f32_16x16x32_bf16 v[52:55], a[84:87], v[4:7], v[52:55]\n\t"
    "v_exp_f32 v37, v37\n\t"
    "v_mfma_f32_16x16x32_bf16 v[52:55], a[88:91], v[8:11], v[52:55]\n\t"
    "v_exp_f32 v38, v38\n\t"
    "v_permlane32_swap_b32 v108, v112\n\t"
    "ds_write_b32 v119, v108 offset:0\n\t"
    "v_mfma_f32_16x16x32_bf16 v[52:55], a[92:95], v[12:15], v[52:55]\n\t"
    "v_exp_f32 v39, v39\n\t"
    "v_mfma_f32_16x16x32_bf16 v[56:59], a[96:99], v[0:3], 0\n\t"
    "v_exp_f32 v40, v40\n\t"
    "v_mfma_f32_16x16x32_bf16 v[56:59], a[100:103], v[4:7], v[56:59]\n\t"
    "v_exp_f32 v41, v41\n\t"
    "v_mfma_f32_16x16x32_bf16 v[56:59], a[104:107], v[8:11], v[56:59]\n\t"
    "v_exp_f32 v42, v42\n\t"
    "v_mfma_f32_16x16x32_bf16 v[56:59], a[108:111], v[12:15], v[56:59]\n\t"
    "v_exp_f32 v43, v43\n\t"
    "v_mfma_f32_16x16x32_bf16 v[60:63], a[112:115], v[0:3], 0\n\t"
    "v_exp_f32 v44, v44\n\t"
    "v_mfma_f32_16x16x32_bf16 v[60:63], a[116:119], v[4:7], v[60:63]\n\t"
    "v_exp_f32 v45, v45\n\t"
    "v_mfma_f32_16x16x32_bf16 v[60:63], a[120:123], v[8:11], v[60:63]\n\t"
    "v_exp_f32 v46, v46\n\t"
    "v_mfma_f32_16x16x32_bf16 v[60:63], a[124:127], v[12:15], v[60:63]\n\t"
    "v_exp_f32 v47, v47\n\t"
    "s_waitcnt lgkmcnt(0)\n\t"
    "s_barrier\n\t"
    "s_add_u32 s53, s53, 0x2000\n\t"
    "s_and_b32 s53, s53, 0xffff\n\t"
    "v_add_u32 v116, s53, v117\n\t"
    "v_cvt_pk_bf16_f32 v96, v32, v33\n\t"
    "v_cvt_pk_bf16_f32 v97, v34, v35\n\t"
    "v_cvt_pk_bf16_f32 v98, v36, v37\n\t"
    "v_cvt_pk_bf16_f32 v99, v38, v39\n\t"
    "ds_write_b128 v118, v[96:99] offset:32768\n\t"
    "v_cvt_pk_bf16_f32 v100, v40, v41\n\t"
    "v_cvt_pk_bf16_f32 v101, v42, v43\n\t"
    "v_cvt_pk_bf16_f32 v102, v44, v45\n\t"
    "v_cvt_pk_bf16_f32 v103, v46, v47\n\t"
    "ds_write_b128 v118, v[100:103] offset:33792\n\t"
    "ds_read_b128 v[0:3], v116 offset:0\n\t"
    "ds_read_b128 v[4:7], v116 offset:512\n\t"
    "v_mfma_f32_16x16x32_bf16 v[108:111], v[104:107], v[96:99], 0\n\t"
    "v_mfma_f32_16x16x32_bf16 v[108:111], v[104:107], v[100:103], v[108:111]\n\t"
    "v_mfma_f32_16x16x32_bf16 v[32:35], a[0:3], v[16:19], 0\n\t"
    "v_exp_f32 v48, v48\n\t"
    "v_mfma_f32_16x16x32_bf16 v[32:35], a[4:7], v[20:23], v[32:35]\n\t"
    "v_exp_f32 v49, v49\n\t"
    "v_mfma_f32_16x16x32_bf16 v[32:35], a[8:11], v[24:27], v[32:35]\n\t"
    "v_exp_f32 v50, v50\n\t"
    "v_mfma_f32_16x16x32_bf16 v[32:35], a[12:15], v[28:31], v[32:35]\n\t"
    "v_exp_f32 v51, v51\n\t"
    "v_mfma_f32_16x16x32_bf16 v[36:39], a[16:19], v[16:19], 0\n\t"
    "v_exp_f32 v52, v52\n\t"
    "v_mfma_f32_16x16x32_bf16 v[36:39], a[20:23], v[20:23], v[36:39]\n\t"
    "v_exp_f32 v53, v53\n\t"
    "v_mfma_f32_16x16x32_bf16 v[36:39], a[24:27], v[24:27], v[36:39]\n\t"
    "v_exp_f32 v54, v54\n\t"
    "v_mfma_f32_16x16x32_bf16 v[36:39], a[28:31], v[28:31], v[36:39]\n\t"
    "v_exp_f32 v55, v55\n\t"
    "v_mfma_f32_16x16x32_bf16 v[40:43], a[32:35], v[16:19], 0\n\t"
    "v_exp_f32 v56, v56\n\t"
    "v_mfma_f32_16x16x32_bf16 v[40:43], a[36:39], v[20:23], v[40:43]\n\t"
    "v_exp_f32 v57, v57\n\t"
    "v_mfma_f32_16x16x32_bf16 v[40:43], a[40:43], v[24:27], v[40:43]\n\t"
    "v_exp_f32 v58, v58\n\t"
    "v_mfma_f32_16x16x32_bf16 v[40:43], a[44:47], v[28:31], v[40:43]\n\t"
    "v_exp_f32 v59, v59\n\t"
    "v_mfma_f32_16x16x32_bf16 v[44:47], a[48:51], v[16:19], 0\n\t"
    "v_exp_f32 v60, v60\n\t"
    "v_mfma_f32_16x16x32_bf16 v[44:47], a[52:55], v[20:23], v[44:47]\n\t"
    "v_exp_f32 v61, v61\n\t"
    "v_mfma_f32_16x16x32_bf16 v[44:47], a[56:59], v[24:27], v[44:47]\n\t"
    "v_exp_f32 v62, v62\n\t"
    "v_mfma_f32_16x16x32_bf16 v[44:47], a[60:63], v[28:31], v[44:47]\n\t"
    "v_exp_f32 v63, v63\n\t"
    "v_cvt_pk_bf16_f32 v96, v48, v49\n\t"
    "v_cvt_pk_bf16_f32 v97, v50, v51\n\t"
    "v_cvt_pk_bf16_f32 v98, v52, v53\n\t"
    "v_cvt_pk_bf16_f32 v99, v54, v55\n\t"
    "ds_write_b128 v118, v[96:99] offset:34816\n\t"
    "v_cvt_pk_bf16_f32 v100, v56, v57\n\t"
    "v_cvt_pk_bf16_f32 v101, v58, v59\n\t"
    "v_cvt_pk_bf16_f32 v102, v60, v61\n\t"
    "v_cvt_pk_bf16_f32 v103, v62, v63\n\t"
    "ds_write_b128 v118, v[100:103] offset:35840\n\t"
    "ds_read_b128 v[8:11], v116 offset:1024\n\t"
    "ds_read_b128 v[12:15], v116 offset:1536\n\t"
    "v_mfma_f32_16x16x32_bf16 v[108:111], v[104:107], v[96:99], v[108:111]\n\t"
    "v_mfma_f32_16x16x32_bf16 v[108:111], v[104:107], v[100:103], v[108:111]\n\t"
    "v_mfma_f32_16x16x32_bf16 v[48:51], a[64:67], v[16:19], 0\n\t"
    "v_exp_f32 v32, v32\n\t"
    "v_mfma_f32_16x16x32_bf16 v[48:51], a[68:71], v[20:23], v[48:51]\n\t"
    "v_exp_f32 v33, v33\n\t"
    "v_mfma_f32_16x16x32_bf16 v[48:51], a[72:75], v[24:27], v[48:51]\n\t"
    "v_exp_f32 v34, v34\n\t"
    "v_mfma_f32_16x16x32_bf16 v[48:51], a[76:79], v[28:31], v[48:51]\n\t"
    "v_exp_f32 v35, v35\n\t"
    "v_mfma_f32_16x16x32_bf16 v[52:55], a[80:83], v[16:19], 0\n\t"
    "v_exp_f32 v36, v36\n\t"
    "v_mfma_f32_16x16x32_bf16 v[52:55], a[84:87], v[20:23], v[52:55]\n\t"
    "v_exp_f32 v37, v37\n\t"
    "v_mfma_f32_16x16x32_bf16 v[52:55], a[88:91], v[24:27], v[52:55]\n\t"
    "v_exp_f32 v38, v38\n\t"
    "v_mfma_f32_16x16x32_bf16 v[52:55], a[92:95], v[28:31], v[52:55]\n\t"
    "v_exp_f32 v39, v39\n\t"
    "v_mfma_f32_16x16x32_bf16 v[56:59], a[96:99], v[16:19], 0\n\t"
    "v_exp_f32 v40, v40\n\t"
    "v_mfma_f32_16x16x32_bf16 v[56:59], a[100:103], v[20:23], v[56:59]\n\t"
    "v_exp_f32 v41, v41\n\t"
    "v_mfma_f32_16x16x32_bf16 v[56:59], a[104:107], v[24:27], v[56:59]\n\t"
    "v_exp_f32 v42, v42\n\t"
    "v_mfma_f32_16x16x32_bf16 v[56:59], a[108:111], v[28:31], v[56:59]\n\t"
    "v_exp_f32 v43, v43\n\t"
    "v_mfma_f32_16x16x32_bf16 v[60:63], a[112:115], v[16:19], 0\n\t"
    "v_exp_f32 v44, v44\n\t"
    "v_mfma_f32_16x16x32_bf16 v[60:63], a[116:119], v[20:23], v[60:63]\n\t"
    "v_exp_f32 v45, v45\n\t"
    "v_mfma_f32_16x16x32_bf16 v[60:63], a[120:123], v[24:27], v[60:63]\n\t"
    "v_exp_f32 v46, v46\n\t"
    "v_mfma_f32_16x16x32_bf16 v[60:63], a[124:127], v[28:31], v[60:63]\n\t"
    "v_exp_f32 v47, v47\n\t"
    "v_cvt_pk_bf16_f32 v96, v32, v33\n\t"
    "v_cvt_pk_bf16_f32 v97, v34, v35\n\t"
    "v_cvt_pk_bf16_f32 v98, v36, v37\n\t"
    "v_cvt_pk_bf16_f32 v99, v38, v39\n\t"
    "ds_write_b128 v118, v[96:99] offset:36864\n\t"
    "v_cvt_pk_bf16_f32 v100, v40, v41\n\t"
    "v_cvt_pk_bf16_f32 v101, v42, v43\n\t"
    "v_cvt_pk_bf16_f32 v102, v44, v45\n\t"
    "v_cvt_pk_bf16_f32 v103, v46, v47\n\t"
    "ds_write_b128 v118, v[100:103] offset:37888\n\t"
    "ds_read_b128 v[16:19], v116 offset:4096\n\t"
    "ds_read_b128 v[20:23], v116 offset:4608\n\t"
    "v_mfma_f32_16x16x32_bf16 v[112:115], v[104:107], v[96:99], 0\n\t"
    "v_mfma_f32_16x16x32_bf16 v[112:115], v[104:107], v[100:103], v[112:115]\n\t"
    "s_waitcnt lgkmcnt(9)\n\t"
    "v_mfma_f32_16x16x32_bf16 v[32:35], a[0:3], v[0:3], 0\n\t"
    "v_exp_f32 v48, v48\n\t"
    "s_waitcnt lgkmcnt(8)\n\t"
    "v_mfma_f32_16x16x32_bf16 v[32:35], a[4:7], v[4:7], v[32:35]\n\t"
    "v_exp_f32 v49, v49\n\t"
    "s_waitcnt lgkmcnt(5)\n\t"
    "v_mfma_f32_16x16x32_bf16 v[32:35], a[8:11], v[8:11], v[32:35]\n\t"
    "v_exp_f32 v50, v50\n\t"
    "s_waitcnt lgkmcnt(4)\n\t"
    "v_mfma_f32_16x16x32_bf16 v[32:35], a[12:15], v[12:15], v[32:35]\n\t"
    "v_exp_f32 v51, v51\n\t"
    "v_mfma_f32_16x16x32_bf16 v[36:39], a[16:19], v[0:3], 0\n\t"
    "v_exp_f32 v52, v52\n\t"
    "v_mfma_f32_16x16x32_bf16 v[36:39], a[20:23], v[4:7], v[36:39]\n\t"
    "v_exp_f32 v53, v53\n\t"
    "v_mfma_f32_16x16x32_bf16 v[36:39], a[24:27], v[8:11], v[36:39]\n\t"
    "v_exp_f32 v54, v54\n\t"
    "v_mfma_f32_16x16x32_bf16 v[36:39], a[28:31], v[12:15], v[36:39]\n\t"
    "v_exp_f32 v55, v55\n\t"
    "v_mfma_f32_16x16x32_bf16 v[40:43], a[32:35], v[0:3], 0\n\t"
    "v_exp_f32 v56, v56\n\t"
    "v_mfma_f32_16x16x32_bf16 v[40:43], a[36:39], v[4:7], v[40:43]\n\t"
    "v_exp_f32 v57, v57\n\t"
    "v_mfma_f32_16x16x32_bf16 v[40:43], a[40:43], v[8:11], v[40:43]\n\t"
    "v_exp_f32 v58, v58\n\t"
    "v_mfma_f32_16x16x32_bf16 v[40:43], a[44:47], v[12:15], v[40:43]\n\t"
    "v_exp_f32 v59, v59\n\t"
    "v_mfma_f32_16x16x32_bf16 v[44:47], a[48:51], v[0:3], 0\n\t"
    "v_exp_f32 v60, v60\n\t"
    "v_mfma_f32_16x16x32_bf16 v[44:47], a[52:55], v[4:7], v[44:47]\n\t"
    "v_exp_f32 v61, v61\n\t"
    "v_mfma_f32_16x16x32_bf16 v[44:47], a[56:59], v[8:11], v[44:47]\n\t"
    "v_exp_f32 v62, v62\n\t"
    "v_mfma_f32_16x16x32_bf16 v[44:47], a[60:63], v[12:15], v[44:47]\n\t"
    "v_exp_f32 v63, v63\n\t"
    "v_cvt_pk_bf16_f32 v96, v48, v49\n\t"
    "v_cvt_pk_bf16_f32 v97, v50, v51\n\t"
    "v_cvt_pk_bf16_f32 v98, v52, v53\n\t"
    "v_cvt_pk_bf16_f32 v99, v54, v55\n\t"
    "ds_write_b128 v118, v[96:99] offset:38912\n\t"
    "v_cvt_pk_bf16_f32 v100, v56, v57\n\t"
    "v_cvt_pk_bf16_f32 v101, v58, v59\n\t"
    "v_cvt_pk_bf16_f32 v102, v60, v61\n\t"
    "v_cvt_pk_bf16_f32 v103, v62, v63\n\t"
    "ds_write_b128 v118, v[100:103] offset:39936\n\t"
    "ds_read_b128 v[24:27], v116 offset:5120\n\t"
    "ds_read_b128 v[28:31], v116 offset:5632\n\t"
    "v_mfma_f32_16x16x32_bf16 v[112:115], v[104:107], v[96:99], v[112:115]\n\t"
    "v_mfma_f32_16x16x32_bf16 v[112:115], v[104:107], v[100:103], v[112:115]\n\t"
    "v_mfma_f32_16x16x32_bf16 v[48:51], a[64:67], v[0:3], 0\n\t"
    "v_exp_f32 v32, v32\n\t"
    "v_mfma_f32_16x16x32_bf16 v[48:51], a[68:71], v[4:7], v[48:51]\n\t"
    "v_exp_f32 v33, v33\n\t"
    "v_mfma_f32_16x16x32_bf16 v[48:51], a[72:75], v[8:11], v[48:51]\n\t"
    "v_exp_f32 v34, v34\n\t"
    "v_mfma_f32_16x16x32_bf16 v[48:51], a[76:79], v[12:15], v[48:51]\n\t"
    "v_exp_f32 v35, v35\n\t"
    "v_mfma_f32_16x16x32_bf16 v[52:55], a[80:83], v[0:3], 0\n\t"
    "v_exp_f32 v36, v36\n\t"
    "v_mfma_f32_16x16x32_bf16 v[52:55], a[84:87], v[4:7], v[52:55]\n\t"
    "v_exp_f32 v37, v37\n\t"
    "v_mfma_f32_16x16x32_bf16 v[52:55], a[88:91], v[8:11], v[52:55]\n\t"
    "v_exp_f32 v38, v38\n\t"
    "v_permlane32_swap_b32 v108, v112\n\t"
    "ds_write_b32 v119, v108 offset:1024\n\t"
    "v_mfma_f32_16x16x32_bf16 v[52:55], a[92:95], v[12:15], v[52:55]\n\t"
    "v_exp_f32 v39, v39\n\t"
    "v_mfma_f32_16x16x32_bf16 v[56:59], a[96:99], v[0:3], 0\n\t"
    "v_exp_f32 v40, v40\n\t"
    "v_mfma_f32_16x16x32_bf16 v[56:59], a[100:103], v[4:7], v[56:59]\n\t"
    "v_exp_f32 v41, v41\n\t"
    "v_mfma_f32_16x16x32_bf16 v[56:59], a[104:107], v[8:11], v[56:59]\n\t"
    "v_exp_f32 v42, v42\n\t"
    "v_mfma_f32_16x16x32_bf16 v[56:59], a[108:111], v[12:15], v[56:59]\n\t"
    "v_exp_f32 v43, v43\n\t"
    "v_mfma_f32_16x16x32_bf16 v[60:63], a[112:115], v[0:3], 0\n\t"
    "v_exp_f32 v44, v44\n\t"
    "v_mfma_f32_16x16x32_bf16 v[60:63], a[116:119], v[4:7], v[60:63]\n\t"
    "v_exp_f32 v45, v45\n\t"
    "v_mfma_f32_16x16x32_bf16 v[60:63], a[120:123], v[8:11], v[60:63]\n\t"
    "v_exp_f32 v46, v46\n\t"
    "v_mfma_f32_16x16x32_bf16 v[60:63], a[124:127], v[12:15], v[60:63]\n\t"
    "v_exp_f32 v47, v47\n\t"
    "s_waitcnt lgkmcnt(0)\n\t"
    "s_barrier\n\t"
    "s_add_u32 s53, s53, 0x2000\n\t"
    "s_and_b32 s53, s53, 0xffff\n\t"
    "v_add_u32 v116, s53, v117\n\t"
    "v_cvt_pk_bf16_f32 v96, v32, v33\n\t"
    "v_cvt_pk_bf16_f32 v97, v34, v35\n\t"
    "v_cvt_pk_bf16_f32 v98, v36, v37\n\t"
    "v_cvt_pk_bf16_f32 v99, v38, v39\n\t"
    "ds_write_b128 v118, v[96:99] offset:0\n\t"
    "v_cvt_pk_bf16_f32 v100, v40, v41\n\t"
    "v_cvt_pk_bf16_f32 v101, v42, v43\n\t"
    "v_cvt_pk_bf16_f32 v102, v44, v45\n\t"
    "v_cvt_pk_bf16_f32 v103, v46, v47\n\t"
    "ds_write_b128 v118, v[100:103] offset:1024\n\t"
    "ds_read_b128 v[0:3], v116 offset:0\n\t"
    "ds_read_b128 v[4:7], v116 offset:512\n\t"
    "v_mfma_f32_16x16x32_bf16 v[108:111], v[104:107], v[96:99], 0\n\t"
    "v_mfma_f32_16x16x32_bf16 v[108:111], v[104:107], v[100:103], v[108:111]\n\t"
    "v_mfma_f32_16x16x32_bf16 v[32:35], a[0:3], v[16:19], 0\n\t"
    "v_exp_f32 v48, v48\n\t"
    "v_mfma_f32_16x16x32_bf16 v[32:35], a[4:7], v[20:23], v[32:35]\n\t"
    "v_exp_f32 v49, v49\n\t"
    "v_mfma_f32_16x16x32_bf16 v[32:35], a[8:11], v[24:27], v[32:35]\n\t"
    "v_exp_f32 v50, v50\n\t"
    "v_mfma_f32_16x16x32_bf16 v[32:35], a[12:15], v[28:31], v[32:35]\n\t"
    "v_exp_f32 v51, v51\n\t"
    "v_mfma_f32_16x16x32_bf16 v[36:39], a[16:19], v[16:19], 0\n\t"
    "v_exp_f32 v52, v52\n\t"
    "v_mfma_f32_16x16x32_bf16 v[36:39], a[20:23], v[20:23], v[36:39]\n\t"
    "v_exp_f32 v53, v53\n\t"
    "v_mfma_f32_16x16x32_bf16 v[36:39], a[24:27], v[24:27], v[36:39]\n\t"
    "v_exp_f32 v54, v54\n\t"
    "v_mfma_f32_16x16x32_bf16 v[36:39], a[28:31], v[28:31], v[36:39]\n\t"
    "v_exp_f32 v55, v55\n\t"
    "v_mfma_f32_16x16x32_bf16 v[40:43], a[32:35], v[16:19], 0\n\t"
    "v_exp_f32 v56, v56\n\t"
    "v_mfma_f32_16x16x32_bf16 v[40:43], a[36:39], v[20:23], v[40:43]\n\t"
    "v_exp_f32 v57, v57\n\t"
    "v_mfma_f32_16x16x32_bf16 v[40:43], a[40:43], v[24:27], v[40:43]\n\t"
    "v_exp_f32 v58, v58\n\t"
    "v_mfma_f32_16x16x32_bf16 v[40:43], a[44:47], v[28:31], v[40:43]\n\t"
    "v_exp_f32 v59, v59\n\t"
    "v_mfma_f32_16x16x32_bf16 v[44:47], a[48:51], v[16:19], 0\n\t"
    "v_exp_f32 v60, v60\n\t"
    "v_mfma_f32_16x16x32_bf16 v[44:47], a[52:55], v[20:23], v[44:47]\n\t"
    "v_exp_f32 v61, v61\n\t"
    "v_mfma_f32_16x16x32_bf16 v[44:47], a[56:59], v[24:27], v[44:47]\n\t"
    "v_exp_f32 v62, v62\n\t"
    "v_mfma_f32_16x16x32_bf16 v[44:47], a[60:63], v[28:31], v[44:47]\n\t"
    "v_exp_f32 v63, v63\n\t"
    "v_cvt_pk_bf16_f32 v96, v48, v49\n\t"
    "v_cvt_pk_bf16_f32 v97, v50, v51\n\t"
    "v_cvt_pk_bf16_f32 v98, v52, v53\n\t"
    "v_cvt_pk_bf16_f32 v99, v54, v55\n\t"
    "ds_write_b128 v118, v[96:99] offset:2048\n\t"
    "v_cvt_pk_bf16_f32 v100, v56, v57\n\t"
    "v_cvt_pk_bf16_f32 v101, v58, v59\n\t"
    "v_cvt_pk_bf16_f32 v102, v60, v61\n\t"
    "v_cvt_pk_bf16_f32 v103, v62, v63\n\t"
    "ds_write_b128 v118, v[100:103] offset:3072\n\t"
    "ds_read_b128 v[8:11], v116 offset:1024\n\t"
    "ds_read_b128 v[12:15], v116 offset:1536\n\t"
    "v_mfma_f32_16x16x32_bf16 v[108:111], v[104:107], v[96:99], v[108:111]\n\t"
    "v_mfma_f32_16x16x32_bf16 v[108:111], v[104:107], v[100:103], v[108:111]\n\t"
    "v_mfma_f32_16x16x32_bf16 v[48:51], a[64:67], v[16:19], 0\n\t"
    "v_exp_f32 v32, v32\n\t"
    "v_mfma_f32_16x16x32_bf16 v[48:51], a[68:71], v[20:23], v[48:51]\n\t"
    "v_exp_f32 v33, v33\n\t"
    "v_mfma_f32_16x16x32_bf16 v[48:51], a[72:75], v[24:27], v[48:51]\n\t"
    "v_exp_f32 v34, v34\n\t"
    "v_mfma_f32_16x16x32_bf16 v[48:51], a[76:79], v[28:31], v[48:51]\n\t"
    "v_exp_f32 v35, v35\n\t"
    "v_mfma_f32_16x16x32_bf16 v[52:55], a[80:83], v[16:19], 0\n\t"
    "v_exp_f32 v36, v36\n\t"
    "v_mfma_f32_16x16x32_bf16 v[52:55], a[84:87], v[20:23], v[52:55]\n\t"
    "v_exp_f32 v37, v37\n\t"
    "v_mfma_f32_16x16x32_bf16 v[52:55], a[88:91], v[24:27], v[52:55]\n\t"
    "v_exp_f32 v38, v38\n\t"
    "v_mfma_f32_16x16x32_bf16 v[52:55], a[92:95], v[28:31], v[52:55]\n\t"
    "v_exp_f32 v39, v39\n\t"
    "v_mfma_f32_16x16x32_bf16 v[56:59], a[96:99], v[16:19], 0\n\t"
    "v_exp_f32 v40, v40\n\t"
    "v_mfma_f32_16x16x32_bf16 v[56:59], a[100:103], v[20:23], v[56:59]\n\t"
    "v_exp_f32 v41, v41\n\t"
    "v_mfma_f32_16x16x32_bf16 v[56:59], a[104:107], v[24:27], v[56:59]\n\t"
    "v_exp_f32 v42, v42\n\t"
    "v_mfma_f32_16x16x32_bf16 v[56:59], a[108:111], v[28:31], v[56:59]\n\t"
    "v_exp_f32 v43, v43\n\t"
    "v_mfma_f32_16x16x32_bf16 v[60:63], a[112:115], v[16:19], 0\n\t"
    "v_exp_f32 v44, v44\n\t"
    "v_mfma_f32_16x16x32_bf16 v[60:63], a[116:119], v[20:23], v[60:63]\n\t"
    "v_exp_f32 v45, v45\n\t"
    "v_mfma_f32_16x16x32_bf16 v[60:63], a[120:123], v[24:27], v[60:63]\n\t"
    "v_exp_f32 v46, v46\n\t"
    "v_mfma_f32_16x16x32_bf16 v[60:63], a[124:127], v[28:31], v[60:63]\n\t"
    "v_exp_f32 v47, v47\n\t"
    "v_cvt_pk_bf16_f32 v96, v32, v33\n\t"
    "v_cvt_pk_bf16_f32 v97, v34, v35\n\t"
    "v_cvt_pk_bf16_f32 v98, v36, v37\n\t"
    "v_cvt_pk_bf16_f32 v99, v38, v39\n\t"
    "ds_write_b128 v118, v[96:99] offset:4096\n\t"
    "v_cvt_pk_bf16_f32 v100, v40, v41\n\t"
    "v_cvt_pk_bf16_f32 v101, v42, v43\n\t"
    "v_cvt_pk_bf16_f32 v102, v44, v45\n\t"
    "v_cvt_pk_bf16_f32 v103, v46, v47\n\t"
    "ds_write_b128 v118, v[100:103] offset:5120\n\t"
    "ds_read_b128 v[16:19], v116 offset:4096\n\t"
    "ds_read_b128 v[20:23], v116 offset:4608\n\t"
    "v_mfma_f32_16x16x32_bf16 v[112:115], v[104:107], v[96:99], 0\n\t"
    "v_mfma_f32_16x16x32_bf16 v[112:115], v[104:107], v[100:103], v[112:115]\n\t"
    "s_waitcnt lgkmcnt(9)\n\t"
    "v_mfma_f32_16x16x32_bf16 v[32:35], a[0:3], v[0:3], 0\n\t"
    "v_exp_f32 v48, v48\n\t"
    "s_waitcnt lgkmcnt(8)\n\t"
    "v_mfma_f32_16x16x32_bf16 v[32:35], a[4:7], v[4:7], v[32:35]\n\t"
    "v_exp_f32 v49, v49\n\t"
    "s_waitcnt lgkmcnt(5)\n\t"
    "v_mfma_f32_16x16x32_bf16 v[32:35], a[8:11], v[8:11], v[32:35]\n\t"
    "v_exp_f32 v50, v50\n\t"
    "s_waitcnt lgkmcnt(4)\n\t"
    "v_mfma_f32_16x16x32_bf16 v[32:35], a[12:15], v[12:15], v[32:35]\n\t"
    "v_exp_f32 v51, v51\n\t"
    "v_mfma_f32_16x16x32_bf16 v[36:39], a[16:19], v[0:3], 0\n\t"
    "v_exp_f32 v52, v52\n\t"
    "v_mfma_f32_16x16x32_bf16 v[36:39], a[20:23], v[4:7], v[36:39]\n\t"
    "v_exp_f32 v53, v53\n\t"
    "v_mfma_f32_16x16x32_bf16 v[36:39], a[24:27], v[8:11], v[36:39]\n\t"
    "v_exp_f32 v54, v54\n\t"
    "v_mfma_f32_16x16x32_bf16 v[36:39], a[28:31], v[12:15], v[36:39]\n\t"
    "v_exp_f32 v55, v55\n\t"
    "v_mfma_f32_16x16x32_bf16 v[40:43], a[32:35], v[0:3], 0\n\t"
    "v_exp_f32 v56, v56\n\t"
    "v_mfma_f32_16x16x32_bf16 v[40:43], a[36:39], v[4:7], v[40:43]\n\t"
    "v_exp_f32 v57, v57\n\t"
    "v_mfma_f32_16x16x32_bf16 v[40:43], a[40:43], v[8:11], v[40:43]\n\t"
    "v_exp_f32 v58, v58\n\t"
    "v_mfma_f32_16x16x32_bf16 v[40:43], a[44:47], v[12:15], v[40:43]\n\t"
    "v_exp_f32 v59, v59\n\t"
    "v_mfma_f32_16x16x32_bf16 v[44:47], a[48:51], v[0:3], 0\n\t"
    "v_exp_f32 v60, v60\n\t"
    "v_mfma_f32_16x16x32_bf16 v[44:47], a[52:55], v[4:7], v[44:47]\n\t"
    "v_exp_f32 v61, v61\n\t"
    "v_mfma_f32_16x16x32_bf16 v[44:47], a[56:59], v[8:11], v[44:47]\n\t"
    "v_exp_f32 v62, v62\n\t"
    "v_mfma_f32_16x16x32_bf16 v[44:47], a[60:63], v[12:15], v[44:47]\n\t"
    "v_exp_f32 v63, v63\n\t"
    "v_cvt_pk_bf16_f32 v96, v48, v49\n\t"
    "v_cvt_pk_bf16_f32 v97, v50, v51\n\t"
    "v_cvt_pk_bf16_f32 v98, v52, v53\n\t"
    "v_cvt_pk_bf16_f32 v99, v54, v55\n\t"
    "ds_write_b128 v118, v[96:99] offset:6144\n\t"
    "v_cvt_pk_bf16_f32 v100, v56, v57\n\t"
    "v_cvt_pk_bf16_f32 v101, v58, v59\n\t"
    "v_cvt_pk_bf16_f32 v102, v60, v61\n\t"
    "v_cvt_pk_bf16_f32 v103, v62, v63\n\t"
    "ds_write_b128 v118, v[100:103] offset:7168\n\t"
    "ds_read_b128 v[24:27], v116 offset:5120\n\t"
    "ds_read_b128 v[28:31], v116 offset:5632\n\t"
    "s_sub_u32 s52, s52, 1\n\t"
    "s_cmp_lg_u32 s52, 0\n\t"
    "s_cbranch_scc1 Lqk_%=\n\t"
    "v_mfma_f32_16x16x32_bf16 v[112:115], v[104:107], v[96:99], v[112:115]\n\t"
    "v_mfma_f32_16x16x32_bf16 v[112:115], v[104:107], v[100:103], v[112:115]\n\t"
    "v_mfma_f32_16x16x32_bf16 v[48:51], a[64:67], v[0:3], 0\n\t"
    "v_exp_f32 v32, v32\n\t"
    "v_mfma_f32_16x16x32_bf16 v[48:51], a[68:71], v[4:7], v[48:51]\n\t"
    "v_exp_f32 v33, v33\n\t"
    "v_mfma_f32_16x16x32_bf16 v[48:51], a[72:75], v[8:11], v[48:51]\n\t"
    "v_exp_f32 v34, v34\n\t"
    "v_mfma_f32_16x16x32_bf16 v[48:51], a[76:79], v[12:15], v[48:51]\n\t"
    "v_exp_f32 v35, v35\n\t"
    "v_mfma_f32_16x16x32_bf16 v[52:55], a[80:83], v[0:3], 0\n\t"
    "v_exp_f32 v36, v36\n\t"
    "v_mfma_f32_16x16x32_bf16 v[52:55], a[84:87], v[4:7], v[52:55]\n\t"
    "v_exp_f32 v37, v37\n\t"
    "v_mfma_f32_16x16x32_bf16 v[52:55], a[88:91], v[8:11], v[52:55]\n\t"
    "v_exp_f32 v38, v38\n\t"
    "v_permlane32_swap_b32 v108, v112\n\t"
    "ds_write_b32 v119, v108 offset:0\n\t"
    "v_mfma_f32_16x16x32_bf16 v[52:55], a[92:95], v[12:15], v[52:55]\n\t"
    "v_exp_f32 v39, v39\n\t"
    "v_mfma_f32_16x16x32_bf16 v[56:59], a[96:99], v[0:3], 0\n\t"
    "v_exp_f32 v40, v40\n\t"
    "v_mfma_f32_16x16x32_bf16 v[56:59], a[100:103], v[4:7], v[56:59]\n\t"
    "v_exp_f32 v41, v41\n\t"
    "v_mfma_f32_16x16x32_bf16 v[56:59], a[104:107], v[8:11], v[56:59]\n\t"
    "v_exp_f32 v42, v42\n\t"
    "v_mfma_f32_16x16x32_bf16 v[56:59], a[108:111], v[12:15], v[56:59]\n\t"
    "v_exp_f32 v43, v43\n\t"
    "v_mfma_f32_16x16x32_bf16 v[60:63], a[112:115], v[0:3], 0\n\t"
    "v_exp_f32 v44, v44\n\t"
    "v_mfma_f32_16x16x32_bf16 v[60:63], a[116:119], v[4:7], v[60:63]\n\t"
    "v_exp_f32 v45, v45\n\t"
    "v_mfma_f32_16x16x32_bf16 v[60:63], a[120:123], v[8:11], v[60:63]\n\t"
    "v_exp_f32 v46, v46\n\t"
    "v_mfma_f32_16x16x32_bf16 v[60:63], a[124:127], v[12:15], v[60:63]\n\t"
    "v_exp_f32 v47, v47\n\t"
    "s_waitcnt lgkmcnt(0)\n\t"
    "s_barrier\n\t"
    "v_cvt_pk_bf16_f32 v96, v32, v33\n\t"
    "v_cvt_pk_bf16_f32 v97, v34, v35\n\t"
    "v_cvt_pk_bf16_f32 v98, v36, v37\n\t"
    "v_cvt_pk_bf16_f32 v99, v38, v39\n\t"
    "ds_write_b128 v118, v[96:99] offset:32768\n\t"
    "v_cvt_pk_bf16_f32 v100, v40, v41\n\t"
    "v_cvt_pk_bf16_f32 v101, v42, v43\n\t"
    "v_cvt_pk_bf16_f32 v102, v44, v45\n\t"
    "v_cvt_pk_bf16_f32 v103, v46, v47\n\t"
    "ds_write_b128 v118, v[100:103] offset:33792\n\t"
    "v_mfma_f32_16x16x32_bf16 v[108:111], v[104:107], v[96:99], 0\n\t"
    "v_mfma_f32_16x16x32_bf16 v[108:111], v[104:107], v[100:103], v[108:111]\n\t"
    "v_mfma_f32_16x16x32_bf16 v[32:35], a[0:3], v[16:19], 0\n\t"
    "v_exp_f32 v48, v48\n\t"
    "v_mfma_f32_16x16x32_bf16 v[32:35], a[4:7], v[20:23], v[32:35]\n\t"
    "v_exp_f32 v49, v49\n\t"
    "v_mfma_f32_16x16x32_bf16 v[32:35], a[8:11], v[24:27], v[32:35]\n\t"
    "v_exp_f32 v50, v50\n\t"
    "v_mfma_f32_16x16x32_bf16 v[32:35], a[12:15], v[28:31], v[32:35]\n\t"
    "v_exp_f32 v51, v51\n\t"
    "v_mfma_f32_16x16x32_bf16 v[36:39], a[16:19], v[16:19], 0\n\t"
    "v_exp_f32 v52, v52\n\t"
    "v_mfma_f32_16x16x32_bf16 v[36:39], a[20:23], v[20:23], v[36:39]\n\t"
    "v_exp_f32 v53, v53\n\t"
    "v_mfma_f32_16x16x32_bf16 v[36:39], a[24:27], v[24:27], v[36:39]\n\t"
    "v_exp_f32 v54, v54\n\t"
    "v_mfma_f32_16x16x32_bf16 v[36:39], a[28:31], v[28:31], v[36:39]\n\t"
    "v_exp_f32 v55, v55\n\t"
    "v_mfma_f32_16x16x32_bf16 v[40:43], a[32:35], v[16:19], 0\n\t"
    "v_exp_f32 v56, v56\n\t"
    "v_mfma_f32_16x16x32_bf16 v[40:43], a[36:39], v[20:23], v[40:43]\n\t"
    "v_exp_f32 v57, v57\n\t"
    "v_mfma_f32_16x16x32_bf16 v[40:43], a[40:43], v[24:27], v[40:43]\n\t"
    "v_exp_f32 v58, v58\n\t"
    "v_mfma_f32_16x16x32_bf16 v[40:43], a[44:47], v[28:31], v[40:43]\n\t"
    "v_exp_f32 v59, v59\n\t"
    "v_mfma_f32_16x16x32_bf16 v[44:47], a[48:51], v[16:19], 0\n\t"
    "v_exp_f32 v60, v60\n\t"
    "v_mfma_f32_16x16x32_bf16 v[44:47], a[52:55], v[20:23], v[44:47]\n\t"
    "v_exp_f32 v61, v61\n\t"
    "v_mfma_f32_16x16x32_bf16 v[44:47], a[56:59], v[24:27], v[44:47]\n\t"
    "v_exp_f32 v62, v62\n\t"
    "v_mfma_f32_16x16x32_bf16 v[44:47], a[60:63], v[28:31], v[44:47]\n\t"
    "v_exp_f32 v63, v63\n\t"
    "v_cvt_pk_bf16_f32 v96, v48, v49\n\t"
    "v_cvt_pk_bf16_f32 v97, v50, v51\n\t"
    "v_cvt_pk_bf16_f32 v98, v52, v53\n\t"
    "v_cvt_pk_bf16_f32 v99, v54, v55\n\t"
    "ds_write_b128 v118, v[96:99] offset:34816\n\t"
    "v_cvt_pk_bf16_f32 v100, v56, v57\n\t"
    "v_cvt_pk_bf16_f32 v101, v58, v59\n\t"
    "v_cvt_pk_bf16_f32 v102, v60, v61\n\t"
    "v_cvt_pk_bf16_f32 v103, v62, v63\n\t"
    "ds_write_b128 v118, v[100:103] offset:35840\n\t"
    "v_mfma_f32_16x16x32_bf16 v[108:111], v[104:107], v[96:99], v[108:111]\n\t"
    "v_mfma_f32_16x16x32_bf16 v[108:111], v[104:107], v[100:103], v[108:111]\n\t"
    "v_mfma_f32_16x16x32_bf16 v[48:51], a[64:67], v[16:19], 0\n\t"
    "v_exp_f32 v32, v32\n\t"
    "v_mfma_f32_16x16x32_bf16 v[48:51], a[68:71], v[20:23], v[48:51]\n\t"
    "v_exp_f32 v33, v33\n\t"
    "v_mfma_f32_16x16x32_bf16 v[48:51], a[72:75], v[24:27], v[48:51]\n\t"
    "v_exp_f32 v34, v34\n\t"
    "v_mfma_f32_16x16x32_bf16 v[48:51], a[76:79], v[28:31], v[48:51]\n\t"
    "v_exp_f32 v35, v35\n\t"
    "v_mfma_f32_16x16x32_bf16 v[52:55], a[80:83], v[16:19], 0\n\t"
    "v_exp_f32 v36, v36\n\t"
    "v_mfma_f32_16x16x32_bf16 v[52:55], a[84:87], v[20:23], v[52:55]\n\t"
    "v_exp_f32 v37, v37\n\t"
    "v_mfma_f32_16x16x32_bf16 v[52:55], a[88:91], v[24:27], v[52:55]\n\t"
    "v_exp_f32 v38, v38\n\t"
    "v_mfma_f32_16x16x32_bf16 v[52:55], a[92:95], v[28:31], v[52:55]\n\t"
    "v_exp_f32 v39, v39\n\t"
    "v_mfma_f32_16x16x32_bf16 v[56:59], a[96:99], v[16:19], 0\n\t"
    "v_exp_f32 v40, v40\n\t"
    "v_mfma_f32_16x16x32_bf16 v[56:59], a[100:103], v[20:23], v[56:59]\n\t"
    "v_exp_f32 v41, v41\n\t"
    "v_mfma_f32_16x16x32_bf16 v[56:59], a[104:107], v[24:27], v[56:59]\n\t"
    "v_exp_f32 v42, v42\n\t"
    "v_mfma_f32_16x16x32_bf16 v[56:59], a[108:111], v[28:31], v[56:59]\n\t"
    "v_exp_f32 v43, v43\n\t"
    "v_mfma_f32_16x16x32_bf16 v[60:63], a[112:115], v[16:19], 0\n\t"
    "v_exp_f32 v44, v44\n\t"
    "v_mfma_f32_16x16x32_bf16 v[60:63], a[116:119], v[20:23], v[60:63]\n\t"
    "v_exp_f32 v45, v45\n\t"
    "v_mfma_f32_16x16x32_bf16 v[60:63], a[120:123], v[24:27], v[60:63]\n\t"
    "v_exp_f32 v46, v46\n\t"
    "v_mfma_f32_16x16x32_bf16 v[60:63], a[124:127], v[28:31], v[60:63]\n\t"
    "v_exp_f32 v47, v47\n\t"
    "v_cvt_pk_bf16_f32 v96, v32, v33\n\t"
    "v_cvt_pk_bf16_f32 v97, v34, v35\n\t"
    "v_cvt_pk_bf16_f32 v98, v36, v37\n\t"
    "v_cvt_pk_bf16_f32 v99, v38, v39\n\t"
    "ds_write_b128 v118, v[96:99] offset:36864\n\t"
    "v_cvt_pk_bf16_f32 v100, v40, v41\n\t"
    "v_cvt_pk_bf16_f32 v101, v42, v43\n\t"
    "v_cvt_pk_bf16_f32 v102, v44, v45\n\t"
    "v_cvt_pk_bf16_f32 v103, v46, v47\n\t"
    "ds_write_b128 v118, v[100:103] offset:37888\n\t"
    "v_mfma_f32_16x16x32_bf16 v[112:115], v[104:107], v[96:99], 0\n\t"
    "v_mfma_f32_16x16x32_bf16 v[112:115], v[104:107], v[100:103], v[112:115]\n\t"
    "v_exp_f32 v48, v48\n\t"
    "v_exp_f32 v49, v49\n\t"
    "v_exp_f32 v50, v50\n\t"
    "v_exp_f32 v51, v51\n\t"
    "v_exp_f32 v52, v52\n\t"
    "v_exp_f32 v53, v53\n\t"
    "v_exp_f32 v54, v54\n\t"
    "v_exp_f32 v55, v55\n\t"
    "v_exp_f32 v56, v56\n\t"
    "v_exp_f32 v57, v57\n\t"
    "v_exp_f32 v58, v58\n\t"
    "v_exp_f32 v59, v59\n\t"
    "v_exp_f32 v60, v60\n\t"
    "v_exp_f32 v61, v61\n\t"
    "v_exp_f32 v62, v62\n\t"
    "v_exp_f32 v63, v63\n\t"
    "v_cvt_pk_bf16_f32 v96, v48, v49\n\t"
    "v_cvt_pk_bf16_f32 v97, v50, v51\n\t"
    "v_cvt_pk_bf16_f32 v98, v52, v53\n\t"
    "v_cvt_pk_bf16_f32 v99, v54, v55\n\t"
    "ds_write_b128 v118, v[96:99] offset:38912\n\t"
    "v_cvt_pk_bf16_f32 v100, v56, v57\n\t"
    "v_cvt_pk_bf16_f32 v101, v58, v59\n\t"
    "v_cvt_pk_bf16_f32 v102, v60, v61\n\t"
    "v_cvt_pk_bf16_f32 v103, v62, v63\n\t"
    "ds_write_b128 v118, v[100:103] offset:39936\n\t"
    "v_mfma_f32_16x16x32_bf16 v[112:115], v[104:107], v[96:99], v[112:115]\n\t"
    "v_mfma_f32_16x16x32_bf16 v[112:115], v[104:107], v[100:103], v[112:115]\n\t"
    "s_nop 7\n\t"
    "v_permlane32_swap_b32 v108, v112\n\t"
    "ds_write_b32 v119, v108 offset:1024\n\t"
    "s_waitcnt lgkmcnt(0)\n\t"
    "s_barrier\n\t"
    "s_barrier\n\t"
    "s_barrier\n\t"
    "s_add_u32 s56, s39, 149504\n\t"
    "v_mov_b32 v100, s56\n\t"
    "ds_read_b64 v[102:103], v100\n\t"
    "s_waitcnt lgkmcnt(0)\n\t"
    "v_readfirstlane_b32 s58, v102\n\t"
    "v_readfirstlane_b32 s59, v103\n\t"
    "s_lshl_b32 s57, s38, 8\n\t"
    "v_mbcnt_lo_u32_b32 v104, -1, 0\n\t"
    "v_mbcnt_hi_u32_b32 v104, -1, v104\n\t"
    "v_lshlrev_b32 v104, 2, v104\n\t"
    "s_add_u32 s56, s39, 149568\n\t"
    "v_add_u32 v105, s56, v104\n\t"
    "v_add_u32 v106, s57, v105\n\t"
    "s_add_u32 s56, s39, 147456\n\t"
    "v_add_u32 v107, s56, v104\n\t"
    "LFBqk_%=:\n\t"
    "s_cmp_eq_u64 s[58:59], 0\n\t"
    "s_cbranch_scc1 LFBXqk_%=\n\t"
    "s_ff1_i32_b64 s68, s[58:59]\n\t"
    "s_bitset0_b64 s[58:59], s68\n\t"
    "s_barrier\n\t"
    "ds_read_b128 v[0:3], v117 offset:0\n\t"
    "ds_read_b128 v[4:7], v117 offset:512\n\t"
    "ds_read_b128 v[8:11], v117 offset:1024\n\t"
    "ds_read_b128 v[12:15], v117 offset:1536\n\t"
    "ds_read_b128 v[16:19], v117 offset:4096\n\t"
    "ds_read_b128 v[20:23], v117 offset:4608\n\t"
    "ds_read_b128 v[24:27], v117 offset:5120\n\t"
    "ds_read_b128 v[28:31], v117 offset:5632\n\t"
    "s_waitcnt lgkmcnt(7)\n\t"
    "v_mfma_f32_16x16x32_bf16 v[32:35], a[0:3], v[0:3], 0\n\t"
    "s_waitcnt lgkmcnt(6)\n\t"
    "v_mfma_f32_16x16x32_bf16 v[32:35], a[4:7], v[4:7], v[32:35]\n\t"
    "s_waitcnt lgkmcnt(5)\n\t"
    "v_mfma_f32_16x16x32_bf16 v[32:35], a[8:11], v[8:11], v[32:35]\n\t"
    "s_waitcnt lgkmcnt(4)\n\t"
    "v_mfma_f32_16x16x32_bf16 v[32:35], a[12:15], v[12:15], v[32:35]\n\t"
    "v_mfma_f32_16x16x32_bf16 v[36:39], a[16:19], v[0:3], 0\n\t"
    "v_mfma_f32_16x16x32_bf16 v[36:39], a[20:23], v[4:7], v[36:39]\n\t"
    "v_mfma_f32_16x16x32_bf16 v[36:39], a[24:27], v[8:11], v[36:39]\n\t"
    "v_mfma_f32_16x16x32_bf16 v[36:39], a[28:31], v[12:15], v[36:39]\n\t"
    "v_mfma_f32_16x16x32_bf16 v[40:43], a[32:35], v[0:3], 0\n\t"
    "v_mfma_f32_16x16x32_bf16 v[40:43], a[36:39], v[4:7], v[40:43]\n\t"
    "v_mfma_f32_16x16x32_bf16 v[40:43], a[40:43], v[8:11], v[40:43]\n\t"
    "v_mfma_f32_16x16x32_bf16 v[40:43], a[44:47], v[12:15], v[40:43]\n\t"
    "v_mfma_f32_16x16x32_bf16 v[44:47], a[48:51], v[0:3], 0\n\t"
    "v_mfma_f32_16x16x32_bf16 v[44:47], a[52:55], v[4:7], v[44:47]\n\t"
    "v_mfma_f32_16x16x32_bf16 v[44:47], a[56:59], v[8:11], v[44:47]\n\t"
    "v_mfma_f32_16x16x32_bf16 v[44:47], a[60:63], v[12:15], v[44:47]\n\t"
    "v_mfma_f32_16x16x32_bf16 v[48:51], a[64:67], v[0:3], 0\n\t"
    "v_mfma_f32_16x16x32_bf16 v[48:51], a[68:71], v[4:7], v[48:51]\n\t"
    "v_mfma_f32_16x16x32_bf16 v[48:51], a[72:75], v[8:11], v[48:51]\n\t"
    "v_mfma_f32_16x16x32_bf16 v[48:51], a[76:79], v[12:15], v[48:51]\n\t"
    "v_mfma_f32_16x16x32_bf16 v[52:55], a[80:83], v[0:3], 0\n\t"
    "v_mfma_f32_16x16x32_bf16 v[52:55], a[84:87], v[4:7], v[52:55]\n\t"
    "v_mfma_f32_16x16x32_bf16 v[52:55], a[88:91], v[8:11], v[52:55]\n\t"
    "v_mfma_f32_16x16x32_bf16 v[52:55], a[92:95], v[12:15], v[52:55]\n\t"
    "v_mfma_f32_16x16x32_bf16 v[56:59], a[96:99], v[0:3], 0\n\t"
    "v_mfma_f32_16x16x32_bf16 v[56:59], a[100:103], v[4:7], v[56:59]\n\t"
    "v_mfma_f32_16x16x32_bf16 v[56:59], a[104:107], v[8:11], v[56:59]\n\t"
    "v_mfma_f32_16x16x32_bf16 v[56:59], a[108:111], v[12:15], v[56:59]\n\t"
    "v_mfma_f32_16x16x32_bf16 v[60:63], a[112:115], v[0:3], 0\n\t"
    "v_mfma_f32_16x16x32_bf16 v[60:63], a[116:119], v[4:7], v[60:63]\n\t"
    "v_mfma_f32_16x16x32_bf16 v[60:63], a[120:123], v[8:11], v[60:63]\n\t"
    "v_mfma_f32_16x16x32_bf16 v[60:63], a[124:127], v[12:15], v[60:63]\n\t"
    "s_waitcnt lgkmcnt(3)\n\t"
    "v_mfma_f32_16x16x32_bf16 v[64:67], a[0:3], v[16:19], 0\n\t"
    "s_waitcnt lgkmcnt(2)\n\t"
    "v_mfma_f32_16x16x32_bf16 v[64:67], a[4:7], v[20:23], v[64:67]\n\t"
    "s_waitcnt lgkmcnt(1)\n\t"
    "v_mfma_f32_16x16x32_bf16 v[64:67], a[8:11], v[24:27], v[64:67]\n\t"
    "s_waitcnt lgkmcnt(0)\n\t"
    "v_mfma_f32_16x16x32_bf16 v[64:67], a[12:15], v[28:31], v[64:67]\n\t"
    "v_mfma_f32_16x16x32_bf16 v[68:71], a[16:19], v[16:19], 0\n\t"
    "v_mfma_f32_16x16x32_bf16 v[68:71], a[20:23], v[20:23], v[68:71]\n\t"
    "v_mfma_f32_16x16x32_bf16 v[68:71], a[24:27], v[24:27], v[68:71]\n\t"
    "v_mfma_f32_16x16x32_bf16 v[68:71], a[28:31], v[28:31], v[68:71]\n\t"
    "v_mfma_f32_16x16x32_bf16 v[72:75], a[32:35], v[16:19], 0\n\t"
    "v_mfma_f32_16x16x32_bf16 v[72:75], a[36:39], v[20:23], v[72:75]\n\t"
    "v_mfma_f32_16x16x32_bf16 v[72:75], a[40:43], v[24:27], v[72:75]\n\t"
    "v_mfma_f32_16x16x32_bf16 v[72:75], a[44:47], v[28:31], v[72:75]\n\t"
    "v_mfma_f32_16x16x32_bf16 v[76:79], a[48:51], v[16:19], 0\n\t"
    "v_mfma_f32_16x16x32_bf16 v[76:79], a[52:55], v[20:23], v[76:79]\n\t"
    "v_mfma_f32_16x16x32_bf16 v[76:79], a[56:59], v[24:27], v[76:79]\n\t"
    "v_mfma_f32_16x16x32_bf16 v[76:79], a[60:63], v[28:31], v[76:79]\n\t"
    "v_mfma_f32_16x16x32_bf16 v[80:83], a[64:67], v[16:19], 0\n\t"
    "v_mfma_f32_16x16x32_bf16 v[80:83], a[68:71], v[20:23], v[80:83]\n\t"
    "v_mfma_f32_16x16x32_bf16 v[80:83], a[72:75], v[24:27], v[80:83]\n\t"
    "v_mfma_f32_16x16x32_bf16 v[80:83], a[76:79], v[28:31], v[80:83]\n\t"
    "v_mfma_f32_16x16x32_bf16 v[84:87], a[80:83], v[16:19], 0\n\t"
    "v_mfma_f32_16x16x32_bf16 v[84:87], a[84:87], v[20:23], v[84:87]\n\t"
    "v_mfma_f32_16x16x32_bf16 v[84:87], a[88:91], v[24:27], v[84:87]\n\t"
    "v_mfma_f32_16x16x32_bf16 v[84:87], a[92:95], v[28:31], v[84:87]\n\t"
    "v_mfma_f32_16x16x32_bf16 v[88:91], a[96:99], v[16:19], 0\n\t"
    "v_mfma_f32_16x16x32_bf16 v[88:91], a[100:103], v[20:23], v[88:91]\n\t"
    "v_mfma_f32_16x16x32_bf16 v[88:91], a[104:107], v[24:27], v[88:91]\n\t"
    "v_mfma_f32_16x16x32_bf16 v[88:91], a[108:111], v[28:31], v[88:91]\n\t"
    "v_mfma_f32_16x16x32_bf16 v[92:95], a[112:115], v[16:19], 0\n\t"
    "v_mfma_f32_16x16x32_bf16 v[92:95], a[116:119], v[20:23], v[92:95]\n\t"
    "v_mfma_f32_16x16x32_bf16 v[92:95], a[120:123], v[24:27], v[92:95]\n\t"
    "v_mfma_f32_16x16x32_bf16 v[92:95], a[124:127], v[28:31], v[92:95]\n\t"
    "v_max3_f32 v96, v32, v33, v34\n\t"
    "v_max3_f32 v96, v96, v35, v36\n\t"
    "v_max3_f32 v96, v96, v37, v38\n\t"
    "v_max3_f32 v96, v96, v39, v40\n\t"
    "v_max3_f32 v96, v96, v41, v42\n\t"
    "v_max3_f32 v96, v96, v43, v44\n\t"
    "v_max3_f32 v96, v96, v45, v46\n\t"
    "v_max3_f32 v96, v96, v47, v48\n\t"
    "v_max3_f32 v96, v96, v49, v50\n\t"
    "v_max3_f32 v96, v96, v51, v52\n\t"
    "v_max3_f32 v96, v96, v53, v54\n\t"
    "v_max3_f32 v96, v96, v55, v56\n\t"
    "v_max3_f32 v96, v96, v57, v58\n\t"
    "v_max3_f32 v96, v96, v59, v60\n\t"
    "v_max3_f32 v96, v96, v61, v62\n\t"
    "v_max_f32 v96, v96, v63\n\t"
    "v_max3_f32 v97, v64, v65, v66\n\t"
    "v_max3_f32 v97, v97, v67, v68\n\t"
    "v_max3_f32 v97, v97, v69, v70\n\t"
    "v_max3_f32 v97, v97, v71, v72\n\t"
    "v_max3_f32 v97, v97, v73, v74\n\t"
    "v_max3_f32 v97, v97, v75, v76\n\t"
    "v_max3_f32 v97, v97, v77, v78\n\t"
    "v_max3_f32 v97, v97, v79, v80\n\t"
    "v_max3_f32 v97, v97, v81, v82\n\t"
    "v_max3_f32 v97, v97, v83, v84\n\t"
    "v_max3_f32 v97, v97, v85, v86\n\t"
    "v_max3_f32 v97, v97, v87, v88\n\t"
    "v_max3_f32 v97, v97, v89, v90\n\t"
    "v_max3_f32 v97, v97, v91, v92\n\t"
    "v_max3_f32 v97, v97, v93, v94\n\t"
    "v_max_f32 v97, v97, v95\n\t"
    "s_nop 1\n\t"
    "v_permlane32_swap_b32 v96, v97\n\t"
    "s_nop 0\n\t"
    "v_max_f32 v98, v96, v97\n\t"
    "v_mov_b32 v99, v98\n\t"
    "s_nop 1\n\t"
    "v_permlane16_swap_b32 v98, v99\n\t"
    "s_nop 0\n\t"
    "v_max_f32 v98, v98, v99\n\t"
    "ds_write_b32 v106, v98\n\t"
    "s_waitcnt lgkmcnt(0)\n\t"
    "s_barrier\n\t"
    "ds_read_b32 v100, v105 offset:0\n\t"
    "ds_read_b32 v101, v105 offset:256\n\t"
    "ds_read_b32 v102, v105 offset:512\n\t"
    "ds_read_b32 v103, v105 offset:768\n\t"
    "s_waitcnt lgkmcnt(1)\n\t"
    "v_max3_f32 v98, v100, v101, v102\n\t"
    "s_waitcnt lgkmcnt(0)\n\t"
    "v_max_f32 v98, v98, v103\n\t"
    "v_mov_b32 v99, v98\n\t"
    "s_nop 1\n\t"
    "v_permlane32_swap_b32 v98, v99\n\t"
    "s_nop 0\n\t"
    "v_sub_f32 v32, v32, v98\n\t"
    "v_sub_f32 v33, v33, v98\n\t"
    "v_sub_f32 v34, v34, v98\n\t"
    "v_sub_f32 v35, v35, v98\n\t"
    "v_sub_f32 v36, v36, v98\n\t"
    "v_sub_f32 v37, v37, v98\n\t"
    "v_sub_f32 v38, v38, v98\n\t"
    "v_sub_f32 v39, v39, v98\n\t"
    "v_sub_f32 v40, v40, v98\n\t"
    "v_sub_f32 v41, v41, v98\n\t"
    "v_sub_f32 v42, v42, v98\n\t"
    "v_sub_f32 v43, v43, v98\n\t"
    "v_sub_f32 v44, v44, v98\n\t"
    "v_sub_f32 v45, v45, v98\n\t"
    "v_sub_f32 v46, v46, v98\n\t"
    "v_sub_f32 v47, v47, v98\n\t"
    "v_sub_f32 v48, v48, v98\n\t"
    "v_sub_f32 v49, v49, v98\n\t"
    "v_sub_f32 v50, v50, v98\n\t"
    "v_sub_f32 v51, v51, v98\n\t"
    "v_sub_f32 v52, v52, v98\n\t"
    "v_sub_f32 v53, v53, v98\n\t"
    "v_sub_f32 v54, v54, v98\n\t"
    "v_sub_f32 v55, v55, v98\n\t"
    "v_sub_f32 v56, v56, v98\n\t"
    "v_sub_f32 v57, v57, v98\n\t"
    "v_sub_f32 v58, v58, v98\n\t"
    "v_sub_f32 v59, v59, v98\n\t"
    "v_sub_f32 v60, v60, v98\n\t"
    "v_sub_f32 v61, v61, v98\n\t"
    "v_sub_f32 v62, v62, v98\n\t"
    "v_sub_f32 v63, v63, v98\n\t"
    "v_exp_f32 v32, v32\n\t"
    "v_exp_f32 v33, v33\n\t"
    "v_exp_f32 v34, v34\n\t"
    "v_exp_f32 v35, v35\n\t"
    "v_exp_f32 v36, v36\n\t"
    "v_exp_f32 v37, v37\n\t"
    "v_exp_f32 v38, v38\n\t"
    "v_exp_f32 v39, v39\n\t"
    "v_exp_f32 v40, v40\n\t"
    "v_exp_f32 v41, v41\n\t"
    "v_exp_f32 v42, v42\n\t"
    "v_exp_f32 v43, v43\n\t"
    "v_exp_f32 v44, v44\n\t"
    "v_exp_f32 v45, v45\n\t"
    "v_exp_f32 v46, v46\n\t"
    "v_exp_f32 v47, v47\n\t"
    "v_exp_f32 v48, v48\n\t"
    "v_exp_f32 v49, v49\n\t"
    "v_exp_f32 v50, v50\n\t"
    "v_exp_f32 v51, v51\n\t"
    "v_exp_f32 v52, v52\n\t"
    "v_exp_f32 v53, v53\n\t"
    "v_exp_f32 v54, v54\n\t"
    "v_exp_f32 v55, v55\n\t"
    "v_exp_f32 v56, v56\n\t"
    "v_exp_f32 v57, v57\n\t"
    "v_exp_f32 v58, v58\n\t"
    "v_exp_f32 v59, v59\n\t"
    "v_exp_f32 v60, v60\n\t"
    "v_exp_f32 v61, v61\n\t"
    "v_exp_f32 v62, v62\n\t"
    "v_exp_f32 v63, v63\n\t"
    "v_add_f32 v96, v32, v33\n\t"
    "v_add_f32 v96, v96, v34\n\t"
    "v_add_f32 v96, v96, v35\n\t"
    "v_add_f32 v96, v96, v36\n\t"
    "v_add_f32 v96, v96, v37\n\t"
    "v_add_f32 v96, v96, v38\n\t"
    "v_add_f32 v96, v96, v39\n\t"
    "v_add_f32 v96, v96, v40\n\t"
    "v_add_f32 v96, v96, v41\n\t"
    "v_add_f32 v96, v96, v42\n\t"
    "v_add_f32 v96, v96, v43\n\t"
    "v_add_f32 v96, v96, v44\n\t"
    "v_add_f32 v96, v96, v45\n\t"
    "v_add_f32 v96, v96, v46\n\t"
    "v_add_f32 v96, v96, v47\n\t"
    "v_add_f32 v96, v96, v48\n\t"
    "v_add_f32 v96, v96, v49\n\t"
    "v_add_f32 v96, v96, v50\n\t"
    "v_add_f32 v96, v96, v51\n\t"
    "v_add_f32 v96, v96, v52\n\t"
    "v_add_f32 v96, v96, v53\n\t"
    "v_add_f32 v96, v96, v54\n\t"
    "v_add_f32 v96, v96, v55\n\t"
    "v_add_f32 v96, v96, v56\n\t"
    "v_add_f32 v96, v96, v57\n\t"
    "v_add_f32 v96, v96, v58\n\t"
    "v_add_f32 v96, v96, v59\n\t"
    "v_add_f32 v96, v96, v60\n\t"
    "v_add_f32 v96, v96, v61\n\t"
    "v_add_f32 v96, v96, v62\n\t"
    "v_add_f32 v96, v96, v63\n\t"
    "v_sub_f32 v64, v64, v99\n\t"
    "v_sub_f32 v65, v65, v99\n\t"
    "v_sub_f32 v66, v66, v99\n\t"
    "v_sub_f32 v67, v67, v99\n\t"
    "v_sub_f32 v68, v68, v99\n\t"
    "v_sub_f32 v69, v69, v99\n\t"
    "v_sub_f32 v70, v70, v99\n\t"
    "v_sub_f32 v71, v71, v99\n\t"
    "v_sub_f32 v72, v72, v99\n\t"
    "v_sub_f32 v73, v73, v99\n\t"
    "v_sub_f32 v74, v74, v99\n\t"
    "v_sub_f32 v75, v75, v99\n\t"
    "v_sub_f32 v76, v76, v99\n\t"
    "v_sub_f32 v77, v77, v99\n\t"
    "v_sub_f32 v78, v78, v99\n\t"
    "v_sub_f32 v79, v79, v99\n\t"
    "v_sub_f32 v80, v80, v99\n\t"
    "v_sub_f32 v81, v81, v99\n\t"
    "v_sub_f32 v82, v82, v99\n\t"
    "v_sub_f32 v83, v83, v99\n\t"
    "v_sub_f32 v84, v84, v99\n\t"
    "v_sub_f32 v85, v85, v99\n\t"
    "v_sub_f32 v86, v86, v99\n\t"
    "v_sub_f32 v87, v87, v99\n\t"
    "v_sub_f32 v88, v88, v99\n\t"
    "v_sub_f32 v89, v89, v99\n\t"
    "v_sub_f32 v90, v90, v99\n\t"
    "v_sub_f32 v91, v91, v99\n\t"
    "v_sub_f32 v92, v92, v99\n\t"
    "v_sub_f32 v93, v93, v99\n\t"
    "v_sub_f32 v94, v94, v99\n\t"
    "v_sub_f32 v95, v95, v99\n\t"
    "v_exp_f32 v64, v64\n\t"
    "v_exp_f32 v65, v65\n\t"
    "v_exp_f32 v66, v66\n\t"
    "v_exp_f32 v67, v67\n\t"
    "v_exp_f32 v68, v68\n\t"
    "v_exp_f32 v69, v69\n\t"
    "v_exp_f32 v70, v70\n\t"
    "v_exp_f32 v71, v71\n\t"
    "v_exp_f32 v72, v72\n\t"
    "v_exp_f32 v73, v73\n\t"
    "v_exp_f32 v74, v74\n\t"
    "v_exp_f32 v75, v75\n\t"
    "v_exp_f32 v76, v76\n\t"
    "v_exp_f32 v77, v77\n\t"
    "v_exp_f32 v78, v78\n\t"
    "v_exp_f32 v79, v79\n\t"
    "v_exp_f32 v80, v80\n\t"
    "v_exp_f32 v81, v81\n\t"
    "v_exp_f32 v82, v82\n\t"
    "v_exp_f32 v83, v83\n\t"
    "v_exp_f32 v84, v84\n\t"
    "v_exp_f32 v85, v85\n\t"
    "v_exp_f32 v86, v86\n\t"
    "v_exp_f32 v87, v87\n\t"
    "v_exp_f32 v88, v88\n\t"
    "v_exp_f32 v89, v89\n\t"
    "v_exp_f32 v90, v90\n\t"
    "v_exp_f32 v91, v91\n\t"
    "v_exp_f32 v92, v92\n\t"
    "v_exp_f32 v93, v93\n\t"
    "v_exp_f32 v94, v94\n\t"
    "v_exp_f32 v95, v95\n\t"
    "v_add_f32 v97, v64, v65\n\t"
    "v_add_f32 v97, v97, v66\n\t"
    "v_add_f32 v97, v97, v67\n\t"
    "v_add_f32 v97, v97, v68\n\t"
    "v_add_f32 v97, v97, v69\n\t"
    "v_add_f32 v97, v97, v70\n\t"
    "v_add_f32 v97, v97, v71\n\t"
    "v_add_f32 v97, v97, v72\n\t"
    "v_add_f32 v97, v97, v73\n\t"
    "v_add_f32 v97, v97, v74\n\t"
    "v_add_f32 v97, v97, v75\n\t"
    "v_add_f32 v97, v97, v76\n\t"
    "v_add_f32 v97, v97, v77\n\t"
    "v_add_f32 v97, v97, v78\n\t"
    "v_add_f32 v97, v97, v79\n\t"
    "v_add_f32 v97, v97, v80\n\t"
    "v_add_f32 v97, v97, v81\n\t"
    "v_add_f32 v97, v97, v82\n\t"
    "v_add_f32 v97, v97, v83\n\t"
    "v_add_f32 v97, v97, v84\n\t"
    "v_add_f32 v97, v97, v85\n\t"
    "v_add_f32 v97, v97, v86\n\t"
    "v_add_f32 v97, v97, v87\n\t"
    "v_add_f32 v97, v97, v88\n\t"
    "v_add_f32 v97, v97, v89\n\t"
    "v_add_f32 v97, v97, v90\n\t"
    "v_add_f32 v97, v97, v91\n\t"
    "v_add_f32 v97, v97, v92\n\t"
    "v_add_f32 v97, v97, v93\n\t"
    "v_add_f32 v97, v97, v94\n\t"
    "v_add_f32 v97, v97, v95\n\t"
    "s_nop 1\n\t"
    "v_permlane32_swap_b32 v96, v97\n\t"
    "s_nop 0\n\t"
    "v_add_f32 v98, v96, v97\n\t"
    "v_mov_b32 v99, v98\n\t"
    "s_nop 1\n\t"
    "v_permlane16_swap_b32 v98, v99\n\t"
    "s_nop 0\n\t"
    "v_add_f32 v98, v98, v99\n\t"
    "ds_write_b32 v119, v98\n\t"
    "s_waitcnt lgkmcnt(0)\n\t"
    "s_barrier\n\t"
    "ds_read_b32 v100, v107 offset:0\n\t"
    "ds_read_b32 v101, v107 offset:256\n\t"
    "ds_read_b32 v102, v107 offset:512\n\t"
    "ds_read_b32 v103, v107 offset:768\n\t"
    "s_waitcnt lgkmcnt(2)\n\t"
    "v_add_f32 v98, v100, v101\n\t"
    "s_waitcnt lgkmcnt(0)\n\t"
    "v_add_f32 v99, v102, v103\n\t"
    "v_add_f32 v98, v98, v99\n\t"
    "v_rcp_f32 v98, v98\n\t"
    "s_nop 0\n\t"
    "v_mov_b32 v99, v98\n\t"
    "s_nop 1\n\t"
    "v_permlane32_swap_b32 v98, v99\n\t"
    "s_nop 0\n\t"
    "v_mul_f32 v32, v32, v98\n\t"
    "v_mul_f32 v33, v33, v98\n\t"
    "v_mul_f32 v34, v34, v98\n\t"
    "v_mul_f32 v35, v35, v98\n\t"
    "v_mul_f32 v36, v36, v98\n\t"
    "v_mul_f32 v37, v37, v98\n\t"
    "v_mul_f32 v38, v38, v98\n\t"
    "v_mul_f32 v39, v39, v98\n\t"
    "v_cvt_pk_bf16_f32 v108, v32, v33\n\t"
    "v_cvt_pk_bf16_f32 v109, v34, v35\n\t"
    "v_cvt_pk_bf16_f32 v110, v36, v37\n\t"
    "v_cvt_pk_bf16_f32 v111, v38, v39\n\t"
    "ds_write_b128 v118, v[108:111] offset:0\n\t"
    "s_waitcnt lgkmcnt(0)\n\t"
    "v_mul_f32 v40, v40, v98\n\t"
    "v_mul_f32 v41, v41, v98\n\t"
    "v_mul_f32 v42, v42, v98\n\t"
    "v_mul_f32 v43, v43, v98\n\t"
    "v_mul_f32 v44, v44, v98\n\t"
    "v_mul_f32 v45, v45, v98\n\t"
    "v_mul_f32 v46, v46, v98\n\t"
    "v_mul_f32 v47, v47, v98\n\t"
    "v_cvt_pk_bf16_f32 v108, v40, v41\n\t"
    "v_cvt_pk_bf16_f32 v109, v42, v43\n\t"
    "v_cvt_pk_bf16_f32 v110, v44, v45\n\t"
    "v_cvt_pk_bf16_f32 v111, v46, v47\n\t"
    "ds_write_b128 v118, v[108:111] offset:1024\n\t"
    "s_waitcnt lgkmcnt(0)\n\t"
    "v_mul_f32 v48, v48, v98\n\t"
    "v_mul_f32 v49, v49, v98\n\t"
    "v_mul_f32 v50, v50, v98\n\t"
    "v_mul_f32 v51, v51, v98\n\t"
    "v_mul_f32 v52, v52, v98\n\t"
    "v_mul_f32 v53, v53, v98\n\t"
    "v_mul_f32 v54, v54, v98\n\t"
    "v_mul_f32 v55, v55, v98\n\t"
    "v_cvt_pk_bf16_f32 v108, v48, v49\n\t"
    "v_cvt_pk_bf16_f32 v109, v50, v51\n\t"
    "v_cvt_pk_bf16_f32 v110, v52, v53\n\t"
    "v_cvt_pk_bf16_f32 v111, v54, v55\n\t"
    "ds_write_b128 v118, v[108:111] offset:2048\n\t"
    "s_waitcnt lgkmcnt(0)\n\t"
    "v_mul_f32 v56, v56, v98\n\t"
    "v_mul_f32 v57, v57, v98\n\t"
    "v_mul_f32 v58, v58, v98\n\t"
    "v_mul_f32 v59, v59, v98\n\t"
    "v_mul_f32 v60, v60, v98\n\t"
    "v_mul_f32 v61, v61, v98\n\t"
    "v_mul_f32 v62, v62, v98\n\t"
    "v_mul_f32 v63, v63, v98\n\t"
    "v_cvt_pk_bf16_f32 v108, v56, v57\n\t"
    "v_cvt_pk_bf16_f32 v109, v58, v59\n\t"
    "v_cvt_pk_bf16_f32 v110, v60, v61\n\t"
    "v_cvt_pk_bf16_f32 v111, v62, v63\n\t"
    "ds_write_b128 v118, v[108:111] offset:3072\n\t"
    "s_waitcnt lgkmcnt(0)\n\t"
    "v_mul_f32 v64, v64, v99\n\t"
    "v_mul_f32 v65, v65, v99\n\t"
    "v_mul_f32 v66, v66, v99\n\t"
    "v_mul_f32 v67, v67, v99\n\t"
    "v_mul_f32 v68, v68, v99\n\t"
    "v_mul_f32 v69, v69, v99\n\t"
    "v_mul_f32 v70, v70, v99\n\t"
    "v_mul_f32 v71, v71, v99\n\t"
    "v_cvt_pk_bf16_f32 v108, v64, v65\n\t"
    "v_cvt_pk_bf16_f32 v109, v66, v67\n\t"
    "v_cvt_pk_bf16_f32 v110, v68, v69\n\t"
    "v_cvt_pk_bf16_f32 v111, v70, v71\n\t"
    "ds_write_b128 v118, v[108:111] offset:4096\n\t"
    "s_waitcnt lgkmcnt(0)\n\t"
    "v_mul_f32 v72, v72, v99\n\t"
    "v_mul_f32 v73, v73, v99\n\t"
    "v_mul_f32 v74, v74, v99\n\t"
    "v_mul_f32 v75, v75, v99\n\t"
    "v_mul_f32 v76, v76, v99\n\t"
    "v_mul_f32 v77, v77, v99\n\t"
    "v_mul_f32 v78, v78, v99\n\t"
    "v_mul_f32 v79, v79, v99\n\t"
    "v_cvt_pk_bf16_f32 v108, v72, v73\n\t"
    "v_cvt_pk_bf16_f32 v109, v74, v75\n\t"
    "v_cvt_pk_bf16_f32 v110, v76, v77\n\t"
    "v_cvt_pk_bf16_f32 v111, v78, v79\n\t"
    "ds_write_b128 v118, v[108:111] offset:5120\n\t"
    "s_waitcnt lgkmcnt(0)\n\t"
    "v_mul_f32 v80, v80, v99\n\t"
    "v_mul_f32 v81, v81, v99\n\t"
    "v_mul_f32 v82, v82, v99\n\t"
    "v_mul_f32 v83, v83, v99\n\t"
    "v_mul_f32 v84, v84, v99\n\t"
    "v_mul_f32 v85, v85, v99\n\t"
    "v_mul_f32 v86, v86, v99\n\t"
    "v_mul_f32 v87, v87, v99\n\t"
    "v_cvt_pk_bf16_f32 v108, v80, v81\n\t"
    "v_cvt_pk_bf16_f32 v109, v82, v83\n\t"
    "v_cvt_pk_bf16_f32 v110, v84, v85\n\t"
    "v_cvt_pk_bf16_f32 v111, v86, v87\n\t"
    "ds_write_b128 v118, v[108:111] offset:6144\n\t"
    "s_waitcnt lgkmcnt(0)\n\t"
    "v_mul_f32 v88, v88, v99\n\t"
    "v_mul_f32 v89, v89, v99\n\t"
    "v_mul_f32 v90, v90, v99\n\t"
    "v_mul_f32 v91, v91, v99\n\t"
    "v_mul_f32 v92, v92, v99\n\t"
    "v_mul_f32 v93, v93, v99\n\t"
    "v_mul_f32 v94, v94, v99\n\t"
    "v_mul_f32 v95, v95, v99\n\t"
    "v_cvt_pk_bf16_f32 v108, v88, v89\n\t"
    "v_cvt_pk_bf16_f32 v109, v90, v91\n\t"
    "v_cvt_pk_bf16_f32 v110, v92, v93\n\t"
    "v_cvt_pk_bf16_f32 v111, v94, v95\n\t"
    "ds_write_b128 v118, v[108:111] offset:7168\n\t"
    "s_waitcnt lgkmcnt(0)\n\t"
    "s_barrier\n\t"
    "s_barrier\n\t"
    "s_branch LFBqk_%=\n\t"
    "LFBXqk_%=:\n\t"
    "s_branch LEND_%=\n\t"
    "LPV_%=:\n\t"
    "s_setprio 3\n\t"
    "s_mov_b64 s[40:41], %0\n\t"
    "s_and_b32 s41, s41, 0xffff\n\t"
    "s_mov_b32 s42, 376832\n\t"
    "s_mov_b32 s43, 0x20000\n\t"
    "s_mov_b64 s[44:45], %2\n\t"
    "s_and_b32 s45, s45, 0xffff\n\t"
    "s_mov_b32 s46, 131072\n\t"
    "s_mov_b32 s47, 0x20000\n\t"
    "s_mov_b64 s[48:49], %3\n\t"
    "s_and_b32 s49, s49, 0xffff\n\t"
    "s_mov_b32 s50, 376832\n\t"
    "s_mov_b32 s51, 0x20000\n\t"
    "v_and_b32 v116, 15, v0\n\t"
    "v_lshrrev_b32 v117, 4, v0\n\t"
    "v_and_b32 v118, 3, v0\n\t"
    "v_lshrrev_b32 v124, 2, v0\n\t"
    "v_and_b32 v124, 7, v124\n\t"
    "v_lshlrev_b32 v124, 8, v124\n\t"
    "v_lshrrev_b32 v125, 5, v0\n\t"
    "v_lshl_add_u32 v124, v125, 6, v124\n\t"
    "s_and_b32 s56, s38, 1\n\t"
    "s_lshl_b32 s56, s56, 1\n\t"
    "v_and_b32 v125, 1, v117\n\t"
    "v_add_u32 v125, s56, v125\n\t"
    "v_sub_u32 v125, 0, v125\n\t"
    "v_and_b32 v125, 3, v125\n\t"
    "v_xor_b32 v125, v125, v118\n\t"
    "v_lshl_add_u32 v120, v125, 4, v124\n\t"
    "s_lshl_b32 s54, s38, 11\n\t"
    "s_add_u32 s65, s54, s39\n\t"
    "s_add_u32 s65, s65, 65536\n\t"
    "s_mov_b32 s53, 0\n\t"
    "s_add_u32 m0, s65, s53\n\t"
    "s_nop 0\n\t"
    "buffer_load_dwordx4 v120, s[40:43], s54 offen lds\n\t"
    "s_add_u32 m0, m0, 0x400\n\t"
    "s_add_u32 s56, s54, 128\n\t"
    "buffer_load_dwordx4 v120, s[40:43], s56 offen lds\n\t"
    "s_add_u32 s54, s54, 0x2000\n\t"
    "s_add_u32 s53, s53, 0x2000\n\t"
    "s_and_b32 s53, s53, 0xffff\n\t"
    "s_add_u32 m0, s65, s53\n\t"
    "s_nop 0\n\t"
    "buffer_load_dwordx4 v120, s[40:43], s54 offen lds\n\t"
    "s_add_u32 m0, m0, 0x400\n\t"
    "s_add_u32 s56, s54, 128\n\t"
    "buffer_load_dwordx4 v120, s[40:43], s56 offen lds\n\t"
    "s_add_u32 s54, s54, 0x2000\n\t"
    "s_add_u32 s53, s53, 0x2000\n\t"
    "s_and_b32 s53, s53, 0xffff\n\t"
    "s_add_u32 m0, s65, s53\n\t"
    "s_nop 0\n\t"
    "buffer_load_dwordx4 v120, s[40:43], s54 offen lds\n\t"
    "s_add_u32 m0, m0, 0x400\n\t"
    "s_add_u32 s56, s54, 128\n\t"
    "buffer_load_dwordx4 v120, s[40:43], s56 offen lds\n\t"
    "s_add_u32 s54, s54, 0x2000\n\t"
    "s_add_u32 s53, s53, 0x2000\n\t"
    "s_and_b32 s53, s53, 0xffff\n\t"
    "s_add_u32 m0, s65, s53\n\t"
    "s_nop 0\n\t"
    "buffer_load_dwordx4 v120, s[40:43], s54 offen lds\n\t"
    "s_add_u32 m0, m0, 0x400\n\t"
    "s_add_u32 s56, s54, 128\n\t"
    "buffer_load_dwordx4 v120, s[40:43], s56 offen lds\n\t"
    "s_add_u32 s54, s54, 0x2000\n\t"
    "s_add_u32 s53, s53, 0x2000\n\t"
    "s_and_b32 s53, s53, 0xffff\n\t"
    "s_add_u32 s56, s39, 149504\n\t"
    "v_mov_b32 v100, s56\n\t"
    "v_mov_b32 v110, 0\n\t"
    "v_mov_b32 v111, 0\n\t"
    "ds_write_b64 v100, v[110:111] offset:0\n\t"
    "ds_write_b64 v100, v[110:111] offset:8\n\t"
    "ds_write_b64 v100, v[110:111] offset:16\n\t"
    "ds_write_b64 v100, v[110:111] offset:24\n\t"
    "s_waitcnt lgkmcnt(0)\n\t"
    "s_barrier\n\t"
    "v_lshlrev_b32 v121, 4, v0\n\t"
    "v_add_u32 v114, s39, v121\n\t"
    "s_add_u32 s56, s39, 147456\n\t"
    "v_lshlrev_b32 v124, 2, v0\n\t"
    "v_add_u32 v115, s56, v124\n\t"
    "v_lshrrev_b32 v124, 5, v0\n\t"
    "v_lshl_add_u32 v124, v124, 4, v116\n\t"
    "v_lshlrev_b32 v124, 2, v124\n\t"
    "s_add_u32 s56, s39, 161792\n\t"
    "v_add_u32 v112, s56, v124\n\t"
    "v_lshlrev_b32 v124, 8, v116\n\t"
    "v_and_b32 v125, 1, v117\n\t"
    "v_lshl_add_u32 v124, v125, 5, v124\n\t"
    "v_lshrrev_b32 v125, 1, v117\n\t"
    "v_lshl_add_u32 v124, v125, 4, v124\n\t"
    "s_lshl_b32 s57, s38, 6\n\t"
    "v_add_u32 v123, s57, v124\n\t"
    "s_mov_b32 s55, 0\n\t"
    "s_mov_b32 s66, 114688\n\t"
    "s_cmp_eq_u32 s38, 1\n\t"
    "s_cselect_b32 s66, 124928, s66\n\t"
    "s_cmp_eq_u32 s38, 2\n\t"
    "s_cselect_b32 s66, 135168, s66\n\t"
    "s_cmp_eq_u32 s38, 3\n\t"
    "s_cselect_b32 s66, 151552, s66\n\t"
    "s_add_u32 s66, s66, s39\n\t"
    "v_lshrrev_b32 v124, 2, v0\n\t"
    "v_lshlrev_b32 v124, 8, v124\n\t"
    "v_and_b32 v125, 1, v117\n\t"
    "v_lshlrev_b32 v125, 1, v125\n\t"
    "v_xor_b32 v125, v125, v118\n\t"
    "v_lshl_add_u32 v124, v125, 4, v124\n\t"
    "s_lshl_b32 s57, s38, 6\n\t"
    "v_add_u32 v96, s57, v124\n\t"
    "v_lshrrev_b32 v124, 2, v116\n\t"
    "v_lshl_add_u32 v124, v117, 2, v124\n\t"
    "v_lshlrev_b32 v124, 6, v124\n\t"
    "v_and_b32 v125, 1, v118\n\t"
    "v_lshl_add_u32 v124, v125, 3, v124\n\t"
    "v_add_u32 v124, s66, v124\n\t"
    "v_lshrrev_b32 v125, 1, v118\n\t"
    "v_and_b32 v126, 1, v117\n\t"
    "v_lshlrev_b32 v126, 1, v126\n\t"
    "v_xor_b32 v125, v125, v126\n\t"
    "v_xor_b32 v126, 0, v125\n\t"
    "v_lshl_add_u32 v97, v126, 4, v124\n\t"
    "v_xor_b32 v126, 2, v125\n\t"
    "v_lshl_add_u32 v98, v126, 4, v124\n\t"
    "v_add_u32 v99, s66, v121\n\t"
    "s_lshl_b32 s57, s38, 2\n\t"
    "s_add_u32 s57, s57, s39\n\t"
    "s_add_u32 s57, s57, 149520\n\t"
    "v_mov_b32 v101, s57\n\t"
    "v_mov_b32 v102, 1\n\t"
    "v_mov_b32 v103, 0\n\t"
    "s_mov_b32 s64, 0x35800000\n\t"
    "s_mov_b64 s[58:59], 0\n\t"
    "s_mov_b64 s[60:61], 1\n\t"
    "s_mov_b32 s67, 0x40000000\n\t"
    "s_waitcnt vmcnt(0)\n\t"
    "s_mov_b64 exec, 1\n\t"
    "ds_add_u32 v100, v102 offset:12\n\t"
    "s_mov_b64 exec, -1\n\t"
    "s_setprio 0\n\t"
    "s_add_u32 m0, s66, 0\n\t"
    "s_mov_b32 s36, 0\n\t"
    "buffer_load_dwordx4 v96, s[44:47], s36 offen lds\n\t"
    "s_add_u32 m0, s66, 1024\n\t"
    "s_mov_b32 s36, 4096\n\t"
    "buffer_load_dwordx4 v96, s[44:47], s36 offen lds\n\t"
    "s_add_u32 m0, s66, 2048\n\t"
    "s_mov_b32 s36, 32768\n\t"
    "buffer_load_dwordx4 v96, s[44:47], s36 offen lds\n\t"
    "s_add_u32 m0, s66, 3072\n\t"
    "s_mov_b32 s36, 36864\n\t"
    "buffer_load_dwordx4 v96, s[44:47], s36 offen lds\n\t"
    "s_add_u32 m0, s66, 4096\n\t"
    "s_mov_b32 s36, 65536\n\t"
    "buffer_load_dwordx4 v96, s[44:47], s36 offen lds\n\t"
    "s_add_u32 m0, s66, 5120\n\t"
    "s_mov_b32 s36, 69632\n\t"
    "buffer_load_dwordx4 v96, s[44:47], s36 offen lds\n\t"
    "s_add_u32 m0, s66, 6144\n\t"
    "s_mov_b32 s36, 98304\n\t"
    "buffer_load_dwordx4 v96, s[44:47], s36 offen lds\n\t"
    "s_add_u32 m0, s66, 7168\n\t"
    "s_mov_b32 s36, 102400\n\t"
    "buffer_load_dwordx4 v96, s[44:47], s36 offen lds\n\t"
    "s_add_u32 m0, s66, 8192\n\t"
    "s_mov_b32 s36, 8192\n\t"
    "buffer_load_dwordx4 v96, s[44:47], s36 offen lds\n\t"
    "s_add_u32 m0, s66, 9216\n\t"
    "s_mov_b32 s36, 12288\n\t"
    "buffer_load_dwordx4 v96, s[44:47], s36 offen lds\n\t"
    "s_waitcnt vmcnt(9)\n\t"
    "ds_read_b64_tr_b16 a[0:1], v97 offset:0\n\t"
    "ds_read_b64_tr_b16 a[4:5], v98 offset:0\n\t"
    "ds_read_b128 v[104:107], v99 offset:0\n\t"
    "s_waitcnt lgkmcnt(0)\n\t"
    "s_add_u32 m0, s66, 0\n\t"
    "s_mov_b32 s36, 40960\n\t"
    "buffer_load_dwordx4 v96, s[44:47], s36 offen lds\n\t"
    "v_and_b32 v104, 0x7fff7fff, v104\n\t"
    "v_pk_max_u16 v103, v103, v104\n\t"
    "v_and_b32 v105, 0x7fff7fff, v105\n\t"
    "v_pk_max_u16 v103, v103, v105\n\t"
    "v_and_b32 v106, 0x7fff7fff, v106\n\t"
    "v_pk_max_u16 v103, v103, v106\n\t"
    "v_and_b32 v107, 0x7fff7fff, v107\n\t"
    "v_pk_max_u16 v103, v103, v107\n\t"
    "s_waitcnt vmcnt(9)\n\t"
    "ds_read_b64_tr_b16 a[2:3], v97 offset:1024\n\t"
    "ds_read_b64_tr_b16 a[6:7], v98 offset:1024\n\t"
    "ds_read_b128 v[104:107], v99 offset:1024\n\t"
    "s_waitcnt lgkmcnt(0)\n\t"
    "s_add_u32 m0, s66, 1024\n\t"
    "s_mov_b32 s36, 45056\n\t"
    "buffer_load_dwordx4 v96, s[44:47], s36 offen lds\n\t"
    "v_and_b32 v104, 0x7fff7fff, v104\n\t"
    "v_pk_max_u16 v103, v103, v104\n\t"
    "v_and_b32 v105, 0x7fff7fff, v105\n\t"
    "v_pk_max_u16 v103, v103, v105\n\t"
    "v_and_b32 v106, 0x7fff7fff, v106\n\t"
    "v_pk_max_u16 v103, v103, v106\n\t"
    "v_and_b32 v107, 0x7fff7fff, v107\n\t"
    "v_pk_max_u16 v103, v103, v107\n\t"
    "s_waitcnt vmcnt(9)\n\t"
    "ds_read_b64_tr_b16 a[32:33], v97 offset:2048\n\t"
    "ds_read_b64_tr_b16 a[36:37], v98 offset:2048\n\t"
    "ds_read_b128 v[104:107], v99 offset:2048\n\t"
    "s_waitcnt lgkmcnt(0)\n\t"
    "s_add_u32 m0, s66, 2048\n\t"
    "s_mov_b32 s36, 73728\n\t"
    "buffer_load_dwordx4 v96, s[44:47], s36 offen lds\n\t"
    "v_and_b32 v104, 0x7fff7fff, v104\n\t"
    "v_pk_max_u16 v103, v103, v104\n\t"
    "v_and_b32 v105, 0x7fff7fff, v105\n\t"
    "v_pk_max_u16 v103, v103, v105\n\t"
    "v_and_b32 v106, 0x7fff7fff, v106\n\t"
    "v_pk_max_u16 v103, v103, v106\n\t"
    "v_and_b32 v107, 0x7fff7fff, v107\n\t"
    "v_pk_max_u16 v103, v103, v107\n\t"
    "s_waitcnt vmcnt(9)\n\t"
    "ds_read_b64_tr_b16 a[34:35], v97 offset:3072\n\t"
    "ds_read_b64_tr_b16 a[38:39], v98 offset:3072\n\t"
    "ds_read_b128 v[104:107], v99 offset:3072\n\t"
    "s_waitcnt lgkmcnt(0)\n\t"
    "s_add_u32 m0, s66, 3072\n\t"
    "s_mov_b32 s36, 77824\n\t"
    "buffer_load_dwordx4 v96, s[44:47], s36 offen lds\n\t"
    "v_and_b32 v104, 0x7fff7fff, v104\n\t"
    "v_pk_max_u16 v103, v103, v104\n\t"
    "v_and_b32 v105, 0x7fff7fff, v105\n\t"
    "v_pk_max_u16 v103, v103, v105\n\t"
    "v_and_b32 v106, 0x7fff7fff, v106\n\t"
    "v_pk_max_u16 v103, v103, v106\n\t"
    "v_and_b32 v107, 0x7fff7fff, v107\n\t"
    "v_pk_max_u16 v103, v103, v107\n\t"
    "s_waitcnt vmcnt(9)\n\t"
    "ds_read_b64_tr_b16 a[64:65], v97 offset:4096\n\t"
    "ds_read_b64_tr_b16 a[68:69], v98 offset:4096\n\t"
    "ds_read_b128 v[104:107], v99 offset:4096\n\t"
    "s_waitcnt lgkmcnt(0)\n\t"
    "s_add_u32 m0, s66, 4096\n\t"
    "s_mov_b32 s36, 106496\n\t"
    "buffer_load_dwordx4 v96, s[44:47], s36 offen lds\n\t"
    "v_and_b32 v104, 0x7fff7fff, v104\n\t"
    "v_pk_max_u16 v103, v103, v104\n\t"
    "v_and_b32 v105, 0x7fff7fff, v105\n\t"
    "v_pk_max_u16 v103, v103, v105\n\t"
    "v_and_b32 v106, 0x7fff7fff, v106\n\t"
    "v_pk_max_u16 v103, v103, v106\n\t"
    "v_and_b32 v107, 0x7fff7fff, v107\n\t"
    "v_pk_max_u16 v103, v103, v107\n\t"
    "s_waitcnt vmcnt(9)\n\t"
    "ds_read_b64_tr_b16 a[66:67], v97 offset:5120\n\t"
    "ds_read_b64_tr_b16 a[70:71], v98 offset:5120\n\t"
    "ds_read_b128 v[104:107], v99 offset:5120\n\t"
    "s_waitcnt lgkmcnt(0)\n\t"
    "s_add_u32 m0, s66, 5120\n\t"
    "s_mov_b32 s36, 110592\n\t"
    "buffer_load_dwordx4 v96, s[44:47], s36 offen lds\n\t"
    "v_and_b32 v104, 0x7fff7fff, v104\n\t"
    "v_pk_max_u16 v103, v103, v104\n\t"
    "v_and_b32 v105, 0x7fff7fff, v105\n\t"
    "v_pk_max_u16 v103, v103, v105\n\t"
    "v_and_b32 v106, 0x7fff7fff, v106\n\t"
    "v_pk_max_u16 v103, v103, v106\n\t"
    "v_and_b32 v107, 0x7fff7fff, v107\n\t"
    "v_pk_max_u16 v103, v103, v107\n\t"
    "s_waitcnt vmcnt(9)\n\t"
    "ds_read_b64_tr_b16 a[96:97], v97 offset:6144\n\t"
    "ds_read_b64_tr_b16 a[100:101], v98 offset:6144\n\t"
    "ds_read_b128 v[104:107], v99 offset:6144\n\t"
    "s_waitcnt lgkmcnt(0)\n\t"
    "s_add_u32 m0, s66, 6144\n\t"
    "s_mov_b32 s36, 16384\n\t"
    "buffer_load_dwordx4 v96, s[44:47], s36 offen lds\n\t"
    "v_and_b32 v104, 0x7fff7fff, v104\n\t"
    "v_pk_max_u16 v103, v103, v104\n\t"
    "v_and_b32 v105, 0x7fff7fff, v105\n\t"
    "v_pk_max_u16 v103, v103, v105\n\t"
    "v_and_b32 v106, 0x7fff7fff, v106\n\t"
    "v_pk_max_u16 v103, v103, v106\n\t"
    "v_and_b32 v107, 0x7fff7fff, v107\n\t"
    "v_pk_max_u16 v103, v103, v107\n\t"
    "s_waitcnt vmcnt(9)\n\t"
    "ds_read_b64_tr_b16 a[98:99], v97 offset:7168\n\t"
    "ds_read_b64_tr_b16 a[102:103], v98 offset:7168\n\t"
    "ds_read_b128 v[104:107], v99 offset:7168\n\t"
    "s_waitcnt lgkmcnt(0)\n\t"
    "s_add_u32 m0, s66, 7168\n\t"
    "s_mov_b32 s36, 20480\n\t"
    "buffer_load_dwordx4 v96, s[44:47], s36 offen lds\n\t"
    "v_and_b32 v104, 0x7fff7fff, v104\n\t"
    "v_pk_max_u16 v103, v103, v104\n\t"
    "v_and_b32 v105, 0x7fff7fff, v105\n\t"
    "v_pk_max_u16 v103, v103, v105\n\t"
    "v_and_b32 v106, 0x7fff7fff, v106\n\t"
    "v_pk_max_u16 v103, v103, v106\n\t"
    "v_and_b32 v107, 0x7fff7fff, v107\n\t"
    "v_pk_max_u16 v103, v103, v107\n\t"
    "s_barrier\n\t"
    "ds_read_b128 v[0:3], v114 offset:0\n\t"
    "ds_read_b128 v[4:7], v114 offset:8192\n\t"
    "ds_read_b128 v[8:11], v114 offset:16384\n\t"
    "ds_read_b128 v[12:15], v114 offset:24576\n\t"
    "ds_read_b128 v[16:19], v114 offset:1024\n\t"
    "ds_read_b128 v[20:23], v114 offset:9216\n\t"
    "ds_read_b128 v[24:27], v114 offset:17408\n\t"
    "ds_read_b128 v[28:31], v114 offset:25600\n\t"
    "s_waitcnt lgkmcnt(7)\n\t"
    "v_mfma_f32_16x16x32_bf16 v[32:35], a[0:3], v[0:3], 0\n\t"
    "s_waitcnt lgkmcnt(6)\n\t"
    "v_mfma_f32_16x16x32_bf16 v[32:35], a[32:35], v[4:7], v[32:35]\n\t"
    "s_waitcnt lgkmcnt(5)\n\t"
    "v_mfma_f32_16x16x32_bf16 v[32:35], a[64:67], v[8:11], v[32:35]\n\t"
    "s_waitcnt lgkmcnt(4)\n\t"
    "v_mfma_f32_16x16x32_bf16 v[32:35], a[96:99], v[12:15], v[32:35]\n\t"
    "v_mfma_f32_16x16x32_bf16 v[40:43], a[4:7], v[0:3], 0\n\t"
    "v_mfma_f32_16x16x32_bf16 v[40:43], a[36:39], v[4:7], v[40:43]\n\t"
    "v_mfma_f32_16x16x32_bf16 v[40:43], a[68:71], v[8:11], v[40:43]\n\t"
    "v_mfma_f32_16x16x32_bf16 v[40:43], a[100:103], v[12:15], v[40:43]\n\t"
    "ds_read_b128 v[0:3], v114 offset:2048\n\t"
    "ds_read_b128 v[4:7], v114 offset:10240\n\t"
    "ds_read_b128 v[8:11], v114 offset:18432\n\t"
    "ds_read_b128 v[12:15], v114 offset:26624\n\t"
    "s_waitcnt lgkmcnt(7)\n\t"
    "v_mfma_f32_16x16x32_bf16 v[36:39], a[0:3], v[16:19], 0\n\t"
    "s_waitcnt lgkmcnt(6)\n\t"
    "v_mfma_f32_16x16x32_bf16 v[36:39], a[32:35], v[20:23], v[36:39]\n\t"
    "s_waitcnt lgkmcnt(5)\n\t"
    "v_mfma_f32_16x16x32_bf16 v[36:39], a[64:67], v[24:27], v[36:39]\n\t"
    "s_waitcnt lgkmcnt(4)\n\t"
    "v_mfma_f32_16x16x32_bf16 v[36:39], a[96:99], v[28:31], v[36:39]\n\t"
    "v_mfma_f32_16x16x32_bf16 v[44:47], a[4:7], v[16:19], 0\n\t"
    "v_mfma_f32_16x16x32_bf16 v[44:47], a[36:39], v[20:23], v[44:47]\n\t"
    "v_mfma_f32_16x16x32_bf16 v[44:47], a[68:71], v[24:27], v[44:47]\n\t"
    "v_mfma_f32_16x16x32_bf16 v[44:47], a[100:103], v[28:31], v[44:47]\n\t"
    "ds_read_b128 v[16:19], v114 offset:3072\n\t"
    "ds_read_b128 v[20:23], v114 offset:11264\n\t"
    "ds_read_b128 v[24:27], v114 offset:19456\n\t"
    "ds_read_b128 v[28:31], v114 offset:27648\n\t"
    "s_waitcnt lgkmcnt(7)\n\t"
    "v_mfma_f32_16x16x32_bf16 v[48:51], a[0:3], v[0:3], 0\n\t"
    "s_waitcnt lgkmcnt(6)\n\t"
    "v_mfma_f32_16x16x32_bf16 v[48:51], a[32:35], v[4:7], v[48:51]\n\t"
    "s_waitcnt lgkmcnt(5)\n\t"
    "v_mfma_f32_16x16x32_bf16 v[48:51], a[64:67], v[8:11], v[48:51]\n\t"
    "s_waitcnt lgkmcnt(4)\n\t"
    "v_mfma_f32_16x16x32_bf16 v[48:51], a[96:99], v[12:15], v[48:51]\n\t"
    "v_mfma_f32_16x16x32_bf16 v[56:59], a[4:7], v[0:3], 0\n\t"
    "v_mfma_f32_16x16x32_bf16 v[56:59], a[36:39], v[4:7], v[56:59]\n\t"
    "v_mfma_f32_16x16x32_bf16 v[56:59], a[68:71], v[8:11], v[56:59]\n\t"
    "v_mfma_f32_16x16x32_bf16 v[56:59], a[100:103], v[12:15], v[56:59]\n\t"
    "ds_read_b128 v[0:3], v114 offset:4096\n\t"
    "ds_read_b128 v[4:7], v114 offset:12288\n\t"
    "ds_read_b128 v[8:11], v114 offset:20480\n\t"
    "ds_read_b128 v[12:15], v114 offset:28672\n\t"
    "s_waitcnt lgkmcnt(7)\n\t"
    "v_mfma_f32_16x16x32_bf16 v[52:55], a[0:3], v[16:19], 0\n\t"
    "s_waitcnt lgkmcnt(6)\n\t"
    "v_mfma_f32_16x16x32_bf16 v[52:55], a[32:35], v[20:23], v[52:55]\n\t"
    "s_waitcnt lgkmcnt(5)\n\t"
    "v_mfma_f32_16x16x32_bf16 v[52:55], a[64:67], v[24:27], v[52:55]\n\t"
    "s_waitcnt lgkmcnt(4)\n\t"
    "v_mfma_f32_16x16x32_bf16 v[52:55], a[96:99], v[28:31], v[52:55]\n\t"
    "v_mfma_f32_16x16x32_bf16 v[60:63], a[4:7], v[16:19], 0\n\t"
    "v_mfma_f32_16x16x32_bf16 v[60:63], a[36:39], v[20:23], v[60:63]\n\t"
    "v_mfma_f32_16x16x32_bf16 v[60:63], a[68:71], v[24:27], v[60:63]\n\t"
    "v_mfma_f32_16x16x32_bf16 v[60:63], a[100:103], v[28:31], v[60:63]\n\t"
    "ds_read_b128 v[16:19], v114 offset:5120\n\t"
    "ds_read_b128 v[20:23], v114 offset:13312\n\t"
    "ds_read_b128 v[24:27], v114 offset:21504\n\t"
    "ds_read_b128 v[28:31], v114 offset:29696\n\t"
    "s_waitcnt lgkmcnt(7)\n\t"
    "v_mfma_f32_16x16x32_bf16 v[64:67], a[0:3], v[0:3], 0\n\t"
    "s_waitcnt lgkmcnt(6)\n\t"
    "v_mfma_f32_16x16x32_bf16 v[64:67], a[32:35], v[4:7], v[64:67]\n\t"
    "s_waitcnt lgkmcnt(5)\n\t"
    "v_mfma_f32_16x16x32_bf16 v[64:67], a[64:67], v[8:11], v[64:67]\n\t"
    "s_waitcnt lgkmcnt(4)\n\t"
    "v_mfma_f32_16x16x32_bf16 v[64:67], a[96:99], v[12:15], v[64:67]\n\t"
    "v_mfma_f32_16x16x32_bf16 v[72:75], a[4:7], v[0:3], 0\n\t"
    "v_mfma_f32_16x16x32_bf16 v[72:75], a[36:39], v[4:7], v[72:75]\n\t"
    "v_mfma_f32_16x16x32_bf16 v[72:75], a[68:71], v[8:11], v[72:75]\n\t"
    "v_mfma_f32_16x16x32_bf16 v[72:75], a[100:103], v[12:15], v[72:75]\n\t"
    "ds_read_b128 v[0:3], v114 offset:6144\n\t"
    "ds_read_b128 v[4:7], v114 offset:14336\n\t"
    "ds_read_b128 v[8:11], v114 offset:22528\n\t"
    "ds_read_b128 v[12:15], v114 offset:30720\n\t"
    "s_waitcnt lgkmcnt(7)\n\t"
    "v_mfma_f32_16x16x32_bf16 v[68:71], a[0:3], v[16:19], 0\n\t"
    "s_waitcnt lgkmcnt(6)\n\t"
    "v_mfma_f32_16x16x32_bf16 v[68:71], a[32:35], v[20:23], v[68:71]\n\t"
    "s_waitcnt lgkmcnt(5)\n\t"
    "v_mfma_f32_16x16x32_bf16 v[68:71], a[64:67], v[24:27], v[68:71]\n\t"
    "s_waitcnt lgkmcnt(4)\n\t"
    "v_mfma_f32_16x16x32_bf16 v[68:71], a[96:99], v[28:31], v[68:71]\n\t"
    "v_mfma_f32_16x16x32_bf16 v[76:79], a[4:7], v[16:19], 0\n\t"
    "v_mfma_f32_16x16x32_bf16 v[76:79], a[36:39], v[20:23], v[76:79]\n\t"
    "v_mfma_f32_16x16x32_bf16 v[76:79], a[68:71], v[24:27], v[76:79]\n\t"
    "v_mfma_f32_16x16x32_bf16 v[76:79], a[100:103], v[28:31], v[76:79]\n\t"
    "ds_read_b128 v[16:19], v114 offset:7168\n\t"
    "ds_read_b128 v[20:23], v114 offset:15360\n\t"
    "ds_read_b128 v[24:27], v114 offset:23552\n\t"
    "ds_read_b128 v[28:31], v114 offset:31744\n\t"
    "s_waitcnt lgkmcnt(7)\n\t"
    "v_mfma_f32_16x16x32_bf16 v[80:83], a[0:3], v[0:3], 0\n\t"
    "s_waitcnt lgkmcnt(6)\n\t"
    "v_mfma_f32_16x16x32_bf16 v[80:83], a[32:35], v[4:7], v[80:83]\n\t"
    "s_waitcnt lgkmcnt(5)\n\t"
    "v_mfma_f32_16x16x32_bf16 v[80:83], a[64:67], v[8:11], v[80:83]\n\t"
    "s_waitcnt lgkmcnt(4)\n\t"
    "v_mfma_f32_16x16x32_bf16 v[80:83], a[96:99], v[12:15], v[80:83]\n\t"
    "v_mfma_f32_16x16x32_bf16 v[88:91], a[4:7], v[0:3], 0\n\t"
    "v_mfma_f32_16x16x32_bf16 v[88:91], a[36:39], v[4:7], v[88:91]\n\t"
    "v_mfma_f32_16x16x32_bf16 v[88:91], a[68:71], v[8:11], v[88:91]\n\t"
    "v_mfma_f32_16x16x32_bf16 v[88:91], a[100:103], v[12:15], v[88:91]\n\t"
    "s_waitcnt lgkmcnt(3)\n\t"
    "v_mfma_f32_16x16x32_bf16 v[84:87], a[0:3], v[16:19], 0\n\t"
    "s_waitcnt lgkmcnt(2)\n\t"
    "v_mfma_f32_16x16x32_bf16 v[84:87], a[32:35], v[20:23], v[84:87]\n\t"
    "s_waitcnt lgkmcnt(1)\n\t"
    "v_mfma_f32_16x16x32_bf16 v[84:87], a[64:67], v[24:27], v[84:87]\n\t"
    "s_waitcnt lgkmcnt(0)\n\t"
    "v_mfma_f32_16x16x32_bf16 v[84:87], a[96:99], v[28:31], v[84:87]\n\t"
    "v_mfma_f32_16x16x32_bf16 v[92:95], a[4:7], v[16:19], 0\n\t"
    "v_mfma_f32_16x16x32_bf16 v[92:95], a[36:39], v[20:23], v[92:95]\n\t"
    "v_mfma_f32_16x16x32_bf16 v[92:95], a[68:71], v[24:27], v[92:95]\n\t"
    "v_mfma_f32_16x16x32_bf16 v[92:95], a[100:103], v[28:31], v[92:95]\n\t"
    "s_waitcnt vmcnt(9)\n\t"
    "ds_read_b64_tr_b16 a[8:9], v97 offset:8192\n\t"
    "ds_read_b64_tr_b16 a[12:13], v98 offset:8192\n\t"
    "ds_read_b128 v[104:107], v99 offset:8192\n\t"
    "s_waitcnt lgkmcnt(0)\n\t"
    "s_add_u32 m0, s66, 8192\n\t"
    "s_mov_b32 s36, 49152\n\t"
    "buffer_load_dwordx4 v96, s[44:47], s36 offen lds\n\t"
    "v_and_b32 v104, 0x7fff7fff, v104\n\t"
    "v_pk_max_u16 v103, v103, v104\n\t"
    "v_and_b32 v105, 0x7fff7fff, v105\n\t"
    "v_pk_max_u16 v103, v103, v105\n\t"
    "v_and_b32 v106, 0x7fff7fff, v106\n\t"
    "v_pk_max_u16 v103, v103, v106\n\t"
    "v_and_b32 v107, 0x7fff7fff, v107\n\t"
    "v_pk_max_u16 v103, v103, v107\n\t"
    "s_waitcnt vmcnt(9)\n\t"
    "ds_read_b64_tr_b16 a[10:11], v97 offset:9216\n\t"
    "ds_read_b64_tr_b16 a[14:15], v98 offset:9216\n\t"
    "ds_read_b128 v[104:107], v99 offset:9216\n\t"
    "s_waitcnt lgkmcnt(0)\n\t"
    "s_add_u32 m0, s66, 9216\n\t"
    "s_mov_b32 s36, 53248\n\t"
    "buffer_load_dwordx4 v96, s[44:47], s36 offen lds\n\t"
    "v_and_b32 v104, 0x7fff7fff, v104\n\t"
    "v_pk_max_u16 v103, v103, v104\n\t"
    "v_and_b32 v105, 0x7fff7fff, v105\n\t"
    "v_pk_max_u16 v103, v103, v105\n\t"
    "v_and_b32 v106, 0x7fff7fff, v106\n\t"
    "v_pk_max_u16 v103, v103, v106\n\t"
    "v_and_b32 v107, 0x7fff7fff, v107\n\t"
    "v_pk_max_u16 v103, v103, v107\n\t"
    "s_waitcnt vmcnt(9)\n\t"
    "ds_read_b64_tr_b16 a[40:41], v97 offset:0\n\t"
    "ds_read_b64_tr_b16 a[44:45], v98 offset:0\n\t"
    "ds_read_b128 v[104:107], v99 offset:0\n\t"
    "s_waitcnt lgkmcnt(0)\n\t"
    "s_add_u32 m0, s66, 0\n\t"
    "s_mov_b32 s36, 81920\n\t"
    "buffer_load_dwordx4 v96, s[44:47], s36 offen lds\n\t"
    "v_and_b32 v104, 0x7fff7fff, v104\n\t"
    "v_pk_max_u16 v103, v103, v104\n\t"
    "v_and_b32 v105, 0x7fff7fff, v105\n\t"
    "v_pk_max_u16 v103, v103, v105\n\t"
    "v_and_b32 v106, 0x7fff7fff, v106\n\t"
    "v_pk_max_u16 v103, v103, v106\n\t"
    "v_and_b32 v107, 0x7fff7fff, v107\n\t"
    "v_pk_max_u16 v103, v103, v107\n\t"
    "s_waitcnt vmcnt(9)\n\t"
    "ds_read_b64_tr_b16 a[42:43], v97 offset:1024\n\t"
    "ds_read_b64_tr_b16 a[46:47], v98 offset:1024\n\t"
    "ds_read_b128 v[104:107], v99 offset:1024\n\t"
    "s_waitcnt lgkmcnt(0)\n\t"
    "s_add_u32 m0, s66, 1024\n\t"
    "s_mov_b32 s36, 86016\n\t"
    "buffer_load_dwordx4 v96, s[44:47], s36 offen lds\n\t"
    "v_and_b32 v104, 0x7fff7fff, v104\n\t"
    "v_pk_max_u16 v103, v103, v104\n\t"
    "v_and_b32 v105, 0x7fff7fff, v105\n\t"
    "v_pk_max_u16 v103, v103, v105\n\t"
    "v_and_b32 v106, 0x7fff7fff, v106\n\t"
    "v_pk_max_u16 v103, v103, v106\n\t"
    "v_and_b32 v107, 0x7fff7fff, v107\n\t"
    "v_pk_max_u16 v103, v103, v107\n\t"
    "s_waitcnt vmcnt(9)\n\t"
    "ds_read_b64_tr_b16 a[72:73], v97 offset:2048\n\t"
    "ds_read_b64_tr_b16 a[76:77], v98 offset:2048\n\t"
    "ds_read_b128 v[104:107], v99 offset:2048\n\t"
    "s_waitcnt lgkmcnt(0)\n\t"
    "s_add_u32 m0, s66, 2048\n\t"
    "s_mov_b32 s36, 114688\n\t"
    "buffer_load_dwordx4 v96, s[44:47], s36 offen lds\n\t"
    "v_and_b32 v104, 0x7fff7fff, v104\n\t"
    "v_pk_max_u16 v103, v103, v104\n\t"
    "v_and_b32 v105, 0x7fff7fff, v105\n\t"
    "v_pk_max_u16 v103, v103, v105\n\t"
    "v_and_b32 v106, 0x7fff7fff, v106\n\t"
    "v_pk_max_u16 v103, v103, v106\n\t"
    "v_and_b32 v107, 0x7fff7fff, v107\n\t"
    "v_pk_max_u16 v103, v103, v107\n\t"
    "s_waitcnt vmcnt(9)\n\t"
    "ds_read_b64_tr_b16 a[74:75], v97 offset:3072\n\t"
    "ds_read_b64_tr_b16 a[78:79], v98 offset:3072\n\t"
    "ds_read_b128 v[104:107], v99 offset:3072\n\t"
    "s_waitcnt lgkmcnt(0)\n\t"
    "s_add_u32 m0, s66, 3072\n\t"
    "s_mov_b32 s36, 118784\n\t"
    "buffer_load_dwordx4 v96, s[44:47], s36 offen lds\n\t"
    "v_and_b32 v104, 0x7fff7fff, v104\n\t"
    "v_pk_max_u16 v103, v103, v104\n\t"
    "v_and_b32 v105, 0x7fff7fff, v105\n\t"
    "v_pk_max_u16 v103, v103, v105\n\t"
    "v_and_b32 v106, 0x7fff7fff, v106\n\t"
    "v_pk_max_u16 v103, v103, v106\n\t"
    "v_and_b32 v107, 0x7fff7fff, v107\n\t"
    "v_pk_max_u16 v103, v103, v107\n\t"
    "s_waitcnt vmcnt(9)\n\t"
    "ds_read_b64_tr_b16 a[104:105], v97 offset:4096\n\t"
    "ds_read_b64_tr_b16 a[108:109], v98 offset:4096\n\t"
    "ds_read_b128 v[104:107], v99 offset:4096\n\t"
    "s_waitcnt lgkmcnt(0)\n\t"
    "s_add_u32 m0, s66, 4096\n\t"
    "s_mov_b32 s36, 24576\n\t"
    "buffer_load_dwordx4 v96, s[44:47], s36 offen lds\n\t"
    "v_and_b32 v104, 0x7fff7fff, v104\n\t"
    "v_pk_max_u16 v103, v103, v104\n\t"
    "v_and_b32 v105, 0x7fff7fff, v105\n\t"
    "v_pk_max_u16 v103, v103, v105\n\t"
    "v_and_b32 v106, 0x7fff7fff, v106\n\t"
    "v_pk_max_u16 v103, v103, v106\n\t"
    "v_and_b32 v107, 0x7fff7fff, v107\n\t"
    "v_pk_max_u16 v103, v103, v107\n\t"
    "s_waitcnt vmcnt(9)\n\t"
    "ds_read_b64_tr_b16 a[106:107], v97 offset:5120\n\t"
    "ds_read_b64_tr_b16 a[110:111], v98 offset:5120\n\t"
    "ds_read_b128 v[104:107], v99 offset:5120\n\t"
    "s_waitcnt lgkmcnt(0)\n\t"
    "s_add_u32 m0, s66, 5120\n\t"
    "s_mov_b32 s36, 28672\n\t"
    "buffer_load_dwordx4 v96, s[44:47], s36 offen lds\n\t"
    "v_and_b32 v104, 0x7fff7fff, v104\n\t"
    "v_pk_max_u16 v103, v103, v104\n\t"
    "v_and_b32 v105, 0x7fff7fff, v105\n\t"
    "v_pk_max_u16 v103, v103, v105\n\t"
    "v_and_b32 v106, 0x7fff7fff, v106\n\t"
    "v_pk_max_u16 v103, v103, v106\n\t"
    "v_and_b32 v107, 0x7fff7fff, v107\n\t"
    "v_pk_max_u16 v103, v103, v107\n\t"
    "s_barrier\n\t"
    "ds_read_b128 v[0:3], v114 offset:32768\n\t"
    "ds_read_b128 v[4:7], v114 offset:40960\n\t"
    "ds_read_b128 v[8:11], v114 offset:49152\n\t"
    "ds_read_b128 v[12:15], v114 offset:57344\n\t"
    "ds_read_b128 v[16:19], v114 offset:33792\n\t"
    "ds_read_b128 v[20:23], v114 offset:41984\n\t"
    "ds_read_b128 v[24:27], v114 offset:50176\n\t"
    "ds_read_b128 v[28:31], v114 offset:58368\n\t"
    "s_waitcnt lgkmcnt(7)\n\t"
    "v_mfma_f32_16x16x32_bf16 v[32:35], a[8:11], v[0:3], v[32:35]\n\t"
    "s_waitcnt lgkmcnt(6)\n\t"
    "v_mfma_f32_16x16x32_bf16 v[32:35], a[40:43], v[4:7], v[32:35]\n\t"
    "s_waitcnt lgkmcnt(5)\n\t"
    "v_mfma_f32_16x16x32_bf16 v[32:35], a[72:75], v[8:11], v[32:35]\n\t"
    "s_waitcnt lgkmcnt(4)\n\t"
    "v_mfma_f32_16x16x32_bf16 v[32:35], a[104:107], v[12:15], v[32:35]\n\t"
    "v_mfma_f32_16x16x32_bf16 v[40:43], a[12:15], v[0:3], v[40:43]\n\t"
    "v_mfma_f32_16x16x32_bf16 v[40:43], a[44:47], v[4:7], v[40:43]\n\t"
    "v_mfma_f32_16x16x32_bf16 v[40:43], a[76:79], v[8:11], v[40:43]\n\t"
    "v_mfma_f32_16x16x32_bf16 v[40:43], a[108:111], v[12:15], v[40:43]\n\t"
    "ds_read_b128 v[0:3], v114 offset:34816\n\t"
    "ds_read_b128 v[4:7], v114 offset:43008\n\t"
    "ds_read_b128 v[8:11], v114 offset:51200\n\t"
    "ds_read_b128 v[12:15], v114 offset:59392\n\t"
    "s_waitcnt lgkmcnt(7)\n\t"
    "v_mfma_f32_16x16x32_bf16 v[36:39], a[8:11], v[16:19], v[36:39]\n\t"
    "s_waitcnt lgkmcnt(6)\n\t"
    "v_mfma_f32_16x16x32_bf16 v[36:39], a[40:43], v[20:23], v[36:39]\n\t"
    "s_waitcnt lgkmcnt(5)\n\t"
    "v_mfma_f32_16x16x32_bf16 v[36:39], a[72:75], v[24:27], v[36:39]\n\t"
    "s_waitcnt lgkmcnt(4)\n\t"
    "v_mfma_f32_16x16x32_bf16 v[36:39], a[104:107], v[28:31], v[36:39]\n\t"
    "v_mfma_f32_16x16x32_bf16 v[44:47], a[12:15], v[16:19], v[44:47]\n\t"
    "v_mfma_f32_16x16x32_bf16 v[44:47], a[44:47], v[20:23], v[44:47]\n\t"
    "v_mfma_f32_16x16x32_bf16 v[44:47], a[76:79], v[24:27], v[44:47]\n\t"
    "v_mfma_f32_16x16x32_bf16 v[44:47], a[108:111], v[28:31], v[44:47]\n\t"
    "ds_read_b128 v[16:19], v114 offset:35840\n\t"
    "ds_read_b128 v[20:23], v114 offset:44032\n\t"
    "ds_read_b128 v[24:27], v114 offset:52224\n\t"
    "ds_read_b128 v[28:31], v114 offset:60416\n\t"
    "s_waitcnt lgkmcnt(7)\n\t"
    "v_mfma_f32_16x16x32_bf16 v[48:51], a[8:11], v[0:3], v[48:51]\n\t"
    "s_waitcnt lgkmcnt(6)\n\t"
    "v_mfma_f32_16x16x32_bf16 v[48:51], a[40:43], v[4:7], v[48:51]\n\t"
    "s_waitcnt lgkmcnt(5)\n\t"
    "v_mfma_f32_16x16x32_bf16 v[48:51], a[72:75], v[8:11], v[48:51]\n\t"
    "s_waitcnt lgkmcnt(4)\n\t"
    "v_mfma_f32_16x16x32_bf16 v[48:51], a[104:107], v[12:15], v[48:51]\n\t"
    "v_mfma_f32_16x16x32_bf16 v[56:59], a[12:15], v[0:3], v[56:59]\n\t"
    "v_mfma_f32_16x16x32_bf16 v[56:59], a[44:47], v[4:7], v[56:59]\n\t"
    "v_mfma_f32_16x16x32_bf16 v[56:59], a[76:79], v[8:11], v[56:59]\n\t"
    "v_mfma_f32_16x16x32_bf16 v[56:59], a[108:111], v[12:15], v[56:59]\n\t"
    "ds_read_b128 v[0:3], v114 offset:36864\n\t"
    "ds_read_b128 v[4:7], v114 offset:45056\n\t"
    "ds_read_b128 v[8:11], v114 offset:53248\n\t"
    "ds_read_b128 v[12:15], v114 offset:61440\n\t"
    "s_waitcnt lgkmcnt(7)\n\t"
    "v_mfma_f32_16x16x32_bf16 v[52:55], a[8:11], v[16:19], v[52:55]\n\t"
    "s_waitcnt lgkmcnt(6)\n\t"
    "v_mfma_f32_16x16x32_bf16 v[52:55], a[40:43], v[20:23], v[52:55]\n\t"
    "s_waitcnt lgkmcnt(5)\n\t"
    "v_mfma_f32_16x16x32_bf16 v[52:55], a[72:75], v[24:27], v[52:55]\n\t"
    "s_waitcnt lgkmcnt(4)\n\t"
    "v_mfma_f32_16x16x32_bf16 v[52:55], a[104:107], v[28:31], v[52:55]\n\t"
    "v_mfma_f32_16x16x32_bf16 v[60:63], a[12:15], v[16:19], v[60:63]\n\t"
    "v_mfma_f32_16x16x32_bf16 v[60:63], a[44:47], v[20:23], v[60:63]\n\t"
    "v_mfma_f32_16x16x32_bf16 v[60:63], a[76:79], v[24:27], v[60:63]\n\t"
    "v_mfma_f32_16x16x32_bf16 v[60:63], a[108:111], v[28:31], v[60:63]\n\t"
    "ds_read_b128 v[16:19], v114 offset:37888\n\t"
    "ds_read_b128 v[20:23], v114 offset:46080\n\t"
    "ds_read_b128 v[24:27], v114 offset:54272\n\t"
    "ds_read_b128 v[28:31], v114 offset:62464\n\t"
    "s_waitcnt lgkmcnt(7)\n\t"
    "v_mfma_f32_16x16x32_bf16 v[64:67], a[8:11], v[0:3], v[64:67]\n\t"
    "s_waitcnt lgkmcnt(6)\n\t"
    "v_mfma_f32_16x16x32_bf16 v[64:67], a[40:43], v[4:7], v[64:67]\n\t"
    "s_waitcnt lgkmcnt(5)\n\t"
    "v_mfma_f32_16x16x32_bf16 v[64:67], a[72:75], v[8:11], v[64:67]\n\t"
    "s_waitcnt lgkmcnt(4)\n\t"
    "v_mfma_f32_16x16x32_bf16 v[64:67], a[104:107], v[12:15], v[64:67]\n\t"
    "v_mfma_f32_16x16x32_bf16 v[72:75], a[12:15], v[0:3], v[72:75]\n\t"
    "v_mfma_f32_16x16x32_bf16 v[72:75], a[44:47], v[4:7], v[72:75]\n\t"
    "v_mfma_f32_16x16x32_bf16 v[72:75], a[76:79], v[8:11], v[72:75]\n\t"
    "v_mfma_f32_16x16x32_bf16 v[72:75], a[108:111], v[12:15], v[72:75]\n\t"
    "ds_read_b128 v[0:3], v114 offset:38912\n\t"
    "ds_read_b128 v[4:7], v114 offset:47104\n\t"
    "ds_read_b128 v[8:11], v114 offset:55296\n\t"
    "ds_read_b128 v[12:15], v114 offset:63488\n\t"
    "s_waitcnt lgkmcnt(7)\n\t"
    "v_mfma_f32_16x16x32_bf16 v[68:71], a[8:11], v[16:19], v[68:71]\n\t"
    "s_waitcnt lgkmcnt(6)\n\t"
    "v_mfma_f32_16x16x32_bf16 v[68:71], a[40:43], v[20:23], v[68:71]\n\t"
    "s_waitcnt lgkmcnt(5)\n\t"
    "v_mfma_f32_16x16x32_bf16 v[68:71], a[72:75], v[24:27], v[68:71]\n\t"
    "s_waitcnt lgkmcnt(4)\n\t"
    "v_mfma_f32_16x16x32_bf16 v[68:71], a[104:107], v[28:31], v[68:71]\n\t"
    "v_mfma_f32_16x16x32_bf16 v[76:79], a[12:15], v[16:19], v[76:79]\n\t"
    "v_mfma_f32_16x16x32_bf16 v[76:79], a[44:47], v[20:23], v[76:79]\n\t"
    "v_mfma_f32_16x16x32_bf16 v[76:79], a[76:79], v[24:27], v[76:79]\n\t"
    "v_mfma_f32_16x16x32_bf16 v[76:79], a[108:111], v[28:31], v[76:79]\n\t"
    "ds_read_b128 v[16:19], v114 offset:39936\n\t"
    "ds_read_b128 v[20:23], v114 offset:48128\n\t"
    "ds_read_b128 v[24:27], v114 offset:56320\n\t"
    "ds_read_b128 v[28:31], v114 offset:64512\n\t"
    "s_waitcnt lgkmcnt(7)\n\t"
    "v_mfma_f32_16x16x32_bf16 v[80:83], a[8:11], v[0:3], v[80:83]\n\t"
    "s_waitcnt lgkmcnt(6)\n\t"
    "v_mfma_f32_16x16x32_bf16 v[80:83], a[40:43], v[4:7], v[80:83]\n\t"
    "s_waitcnt lgkmcnt(5)\n\t"
    "v_mfma_f32_16x16x32_bf16 v[80:83], a[72:75], v[8:11], v[80:83]\n\t"
    "s_waitcnt lgkmcnt(4)\n\t"
    "v_mfma_f32_16x16x32_bf16 v[80:83], a[104:107], v[12:15], v[80:83]\n\t"
    "v_mfma_f32_16x16x32_bf16 v[88:91], a[12:15], v[0:3], v[88:91]\n\t"
    "v_mfma_f32_16x16x32_bf16 v[88:91], a[44:47], v[4:7], v[88:91]\n\t"
    "v_mfma_f32_16x16x32_bf16 v[88:91], a[76:79], v[8:11], v[88:91]\n\t"
    "v_mfma_f32_16x16x32_bf16 v[88:91], a[108:111], v[12:15], v[88:91]\n\t"
    "s_waitcnt lgkmcnt(3)\n\t"
    "v_mfma_f32_16x16x32_bf16 v[84:87], a[8:11], v[16:19], v[84:87]\n\t"
    "s_waitcnt lgkmcnt(2)\n\t"
    "v_mfma_f32_16x16x32_bf16 v[84:87], a[40:43], v[20:23], v[84:87]\n\t"
    "s_waitcnt lgkmcnt(1)\n\t"
    "v_mfma_f32_16x16x32_bf16 v[84:87], a[72:75], v[24:27], v[84:87]\n\t"
    "s_waitcnt lgkmcnt(0)\n\t"
    "v_mfma_f32_16x16x32_bf16 v[84:87], a[104:107], v[28:31], v[84:87]\n\t"
    "v_mfma_f32_16x16x32_bf16 v[92:95], a[12:15], v[16:19], v[92:95]\n\t"
    "v_mfma_f32_16x16x32_bf16 v[92:95], a[44:47], v[20:23], v[92:95]\n\t"
    "v_mfma_f32_16x16x32_bf16 v[92:95], a[76:79], v[24:27], v[92:95]\n\t"
    "v_mfma_f32_16x16x32_bf16 v[92:95], a[108:111], v[28:31], v[92:95]\n\t"
    "s_waitcnt vmcnt(9)\n\t"
    "ds_read_b64_tr_b16 a[16:17], v97 offset:6144\n\t"
    "ds_read_b64_tr_b16 a[20:21], v98 offset:6144\n\t"
    "ds_read_b128 v[104:107], v99 offset:6144\n\t"
    "s_waitcnt lgkmcnt(0)\n\t"
    "s_add_u32 m0, s66, 6144\n\t"
    "s_mov_b32 s36, 57344\n\t"
    "buffer_load_dwordx4 v96, s[44:47], s36 offen lds\n\t"
    "v_and_b32 v104, 0x7fff7fff, v104\n\t"
    "v_pk_max_u16 v103, v103, v104\n\t"
    "v_and_b32 v105, 0x7fff7fff, v105\n\t"
    "v_pk_max_u16 v103, v103, v105\n\t"
    "v_and_b32 v106, 0x7fff7fff, v106\n\t"
    "v_pk_max_u16 v103, v103, v106\n\t"
    "v_and_b32 v107, 0x7fff7fff, v107\n\t"
    "v_pk_max_u16 v103, v103, v107\n\t"
    "s_waitcnt vmcnt(9)\n\t"
    "ds_read_b64_tr_b16 a[18:19], v97 offset:7168\n\t"
    "ds_read_b64_tr_b16 a[22:23], v98 offset:7168\n\t"
    "ds_read_b128 v[104:107], v99 offset:7168\n\t"
    "s_waitcnt lgkmcnt(0)\n\t"
    "s_add_u32 m0, s66, 7168\n\t"
    "s_mov_b32 s36, 61440\n\t"
    "buffer_load_dwordx4 v96, s[44:47], s36 offen lds\n\t"
    "v_and_b32 v104, 0x7fff7fff, v104\n\t"
    "v_pk_max_u16 v103, v103, v104\n\t"
    "v_and_b32 v105, 0x7fff7fff, v105\n\t"
    "v_pk_max_u16 v103, v103, v105\n\t"
    "v_and_b32 v106, 0x7fff7fff, v106\n\t"
    "v_pk_max_u16 v103, v103, v106\n\t"
    "v_and_b32 v107, 0x7fff7fff, v107\n\t"
    "v_pk_max_u16 v103, v103, v107\n\t"
    "s_waitcnt vmcnt(9)\n\t"
    "ds_read_b64_tr_b16 a[48:49], v97 offset:8192\n\t"
    "ds_read_b64_tr_b16 a[52:53], v98 offset:8192\n\t"
    "ds_read_b128 v[104:107], v99 offset:8192\n\t"
    "s_waitcnt lgkmcnt(0)\n\t"
    "s_add_u32 m0, s66, 8192\n\t"
    "s_mov_b32 s36, 90112\n\t"
    "buffer_load_dwordx4 v96, s[44:47], s36 offen lds\n\t"
    "v_and_b32 v104, 0x7fff7fff, v104\n\t"
    "v_pk_max_u16 v103, v103, v104\n\t"
    "v_and_b32 v105, 0x7fff7fff, v105\n\t"
    "v_pk_max_u16 v103, v103, v105\n\t"
    "v_and_b32 v106, 0x7fff7fff, v106\n\t"
    "v_pk_max_u16 v103, v103, v106\n\t"
    "v_and_b32 v107, 0x7fff7fff, v107\n\t"
    "v_pk_max_u16 v103, v103, v107\n\t"
    "s_waitcnt vmcnt(9)\n\t"
    "ds_read_b64_tr_b16 a[50:51], v97 offset:9216\n\t"
    "ds_read_b64_tr_b16 a[54:55], v98 offset:9216\n\t"
    "ds_read_b128 v[104:107], v99 offset:9216\n\t"
    "s_waitcnt lgkmcnt(0)\n\t"
    "s_add_u32 m0, s66, 9216\n\t"
    "s_mov_b32 s36, 94208\n\t"
    "buffer_load_dwordx4 v96, s[44:47], s36 offen lds\n\t"
    "v_and_b32 v104, 0x7fff7fff, v104\n\t"
    "v_pk_max_u16 v103, v103, v104\n\t"
    "v_and_b32 v105, 0x7fff7fff, v105\n\t"
    "v_pk_max_u16 v103, v103, v105\n\t"
    "v_and_b32 v106, 0x7fff7fff, v106\n\t"
    "v_pk_max_u16 v103, v103, v106\n\t"
    "v_and_b32 v107, 0x7fff7fff, v107\n\t"
    "v_pk_max_u16 v103, v103, v107\n\t"
    "s_waitcnt vmcnt(9)\n\t"
    "ds_read_b64_tr_b16 a[80:81], v97 offset:0\n\t"
    "ds_read_b64_tr_b16 a[84:85], v98 offset:0\n\t"
    "ds_read_b128 v[104:107], v99 offset:0\n\t"
    "s_waitcnt lgkmcnt(0)\n\t"
    "s_add_u32 m0, s66, 0\n\t"
    "s_mov_b32 s36, 122880\n\t"
    "buffer_load_dwordx4 v96, s[44:47], s36 offen lds\n\t"
    "v_and_b32 v104, 0x7fff7fff, v104\n\t"
    "v_pk_max_u16 v103, v103, v104\n\t"
    "v_and_b32 v105, 0x7fff7fff, v105\n\t"
    "v_pk_max_u16 v103, v103, v105\n\t"
    "v_and_b32 v106, 0x7fff7fff, v106\n\t"
    "v_pk_max_u16 v103, v103, v106\n\t"
    "v_and_b32 v107, 0x7fff7fff, v107\n\t"
    "v_pk_max_u16 v103, v103, v107\n\t"
    "s_waitcnt vmcnt(9)\n\t"
    "ds_read_b64_tr_b16 a[82:83], v97 offset:1024\n\t"
    "ds_read_b64_tr_b16 a[86:87], v98 offset:1024\n\t"
    "ds_read_b128 v[104:107], v99 offset:1024\n\t"
    "s_waitcnt lgkmcnt(0)\n\t"
    "s_add_u32 m0, s66, 1024\n\t"
    "s_mov_b32 s36, 126976\n\t"
    "buffer_load_dwordx4 v96, s[44:47], s36 offen lds\n\t"
    "s_add_u32 m0, s65, s53\n\t"
    "s_nop 0\n\t"
    "buffer_load_dwordx4 v120, s[40:43], s54 offen lds\n\t"
    "s_add_u32 m0, m0, 0x400\n\t"
    "s_add_u32 s56, s54, 128\n\t"
    "buffer_load_dwordx4 v120, s[40:43], s56 offen lds\n\t"
    "s_add_u32 s54, s54, 0x2000\n\t"
    "s_add_u32 s53, s53, 0x2000\n\t"
    "s_and_b32 s53, s53, 0xffff\n\t"
    "s_add_u32 m0, s65, s53\n\t"
    "s_nop 0\n\t"
    "buffer_load_dwordx4 v120, s[40:43], s54 offen lds\n\t"
    "s_add_u32 m0, m0, 0x400\n\t"
    "s_add_u32 s56, s54, 128\n\t"
    "buffer_load_dwordx4 v120, s[40:43], s56 offen lds\n\t"
    "s_add_u32 s54, s54, 0x2000\n\t"
    "s_add_u32 s53, s53, 0x2000\n\t"
    "s_and_b32 s53, s53, 0xffff\n\t"
    "v_and_b32 v104, 0x7fff7fff, v104\n\t"
    "v_pk_max_u16 v103, v103, v104\n\t"
    "v_and_b32 v105, 0x7fff7fff, v105\n\t"
    "v_pk_max_u16 v103, v103, v105\n\t"
    "v_and_b32 v106, 0x7fff7fff, v106\n\t"
    "v_pk_max_u16 v103, v103, v106\n\t"
    "v_and_b32 v107, 0x7fff7fff, v107\n\t"
    "v_pk_max_u16 v103, v103, v107\n\t"
    "s_waitcnt vmcnt(13)\n\t"
    "ds_read_b64_tr_b16 a[112:113], v97 offset:2048\n\t"
    "ds_read_b64_tr_b16 a[116:117], v98 offset:2048\n\t"
    "ds_read_b128 v[104:107], v99 offset:2048\n\t"
    "s_waitcnt lgkmcnt(0)\n\t"
    "v_and_b32 v104, 0x7fff7fff, v104\n\t"
    "v_pk_max_u16 v103, v103, v104\n\t"
    "v_and_b32 v105, 0x7fff7fff, v105\n\t"
    "v_pk_max_u16 v103, v103, v105\n\t"
    "v_and_b32 v106, 0x7fff7fff, v106\n\t"
    "v_pk_max_u16 v103, v103, v106\n\t"
    "v_and_b32 v107, 0x7fff7fff, v107\n\t"
    "v_pk_max_u16 v103, v103, v107\n\t"
    "s_waitcnt vmcnt(12)\n\t"
    "ds_read_b64_tr_b16 a[114:115], v97 offset:3072\n\t"
    "ds_read_b64_tr_b16 a[118:119], v98 offset:3072\n\t"
    "ds_read_b128 v[104:107], v99 offset:3072\n\t"
    "s_waitcnt lgkmcnt(0)\n\t"
    "v_and_b32 v104, 0x7fff7fff, v104\n\t"
    "v_pk_max_u16 v103, v103, v104\n\t"
    "v_and_b32 v105, 0x7fff7fff, v105\n\t"
    "v_pk_max_u16 v103, v103, v105\n\t"
    "v_and_b32 v106, 0x7fff7fff, v106\n\t"
    "v_pk_max_u16 v103, v103, v106\n\t"
    "v_and_b32 v107, 0x7fff7fff, v107\n\t"
    "v_pk_max_u16 v103, v103, v107\n\t"
    "s_barrier\n\t"
    "ds_read_b128 v[0:3], v114 offset:0\n\t"
    "ds_read_b128 v[4:7], v114 offset:8192\n\t"
    "ds_read_b128 v[8:11], v114 offset:16384\n\t"
    "ds_read_b128 v[12:15], v114 offset:24576\n\t"
    "ds_read_b128 v[16:19], v114 offset:1024\n\t"
    "ds_read_b128 v[20:23], v114 offset:9216\n\t"
    "ds_read_b128 v[24:27], v114 offset:17408\n\t"
    "ds_read_b128 v[28:31], v114 offset:25600\n\t"
    "s_waitcnt lgkmcnt(7)\n\t"
    "v_mfma_f32_16x16x32_bf16 v[32:35], a[16:19], v[0:3], v[32:35]\n\t"
    "s_waitcnt lgkmcnt(6)\n\t"
    "v_mfma_f32_16x16x32_bf16 v[32:35], a[48:51], v[4:7], v[32:35]\n\t"
    "s_waitcnt lgkmcnt(5)\n\t"
    "v_mfma_f32_16x16x32_bf16 v[32:35], a[80:83], v[8:11], v[32:35]\n\t"
    "s_waitcnt lgkmcnt(4)\n\t"
    "v_mfma_f32_16x16x32_bf16 v[32:35], a[112:115], v[12:15], v[32:35]\n\t"
    "v_mfma_f32_16x16x32_bf16 v[40:43], a[20:23], v[0:3], v[40:43]\n\t"
    "v_mfma_f32_16x16x32_bf16 v[40:43], a[52:55], v[4:7], v[40:43]\n\t"
    "v_mfma_f32_16x16x32_bf16 v[40:43], a[84:87], v[8:11], v[40:43]\n\t"
    "v_mfma_f32_16x16x32_bf16 v[40:43], a[116:119], v[12:15], v[40:43]\n\t"
    "ds_read_b128 v[0:3], v114 offset:2048\n\t"
    "ds_read_b128 v[4:7], v114 offset:10240\n\t"
    "ds_read_b128 v[8:11], v114 offset:18432\n\t"
    "ds_read_b128 v[12:15], v114 offset:26624\n\t"
    "s_waitcnt lgkmcnt(7)\n\t"
    "v_mfma_f32_16x16x32_bf16 v[36:39], a[16:19], v[16:19], v[36:39]\n\t"
    "s_waitcnt lgkmcnt(6)\n\t"
    "v_mfma_f32_16x16x32_bf16 v[36:39], a[48:51], v[20:23], v[36:39]\n\t"
    "s_waitcnt lgkmcnt(5)\n\t"
    "v_mfma_f32_16x16x32_bf16 v[36:39], a[80:83], v[24:27], v[36:39]\n\t"
    "s_waitcnt lgkmcnt(4)\n\t"
    "v_mfma_f32_16x16x32_bf16 v[36:39], a[112:115], v[28:31], v[36:39]\n\t"
    "v_mfma_f32_16x16x32_bf16 v[44:47], a[20:23], v[16:19], v[44:47]\n\t"
    "v_mfma_f32_16x16x32_bf16 v[44:47], a[52:55], v[20:23], v[44:47]\n\t"
    "v_mfma_f32_16x16x32_bf16 v[44:47], a[84:87], v[24:27], v[44:47]\n\t"
    "v_mfma_f32_16x16x32_bf16 v[44:47], a[116:119], v[28:31], v[44:47]\n\t"
    "ds_read_b128 v[16:19], v114 offset:3072\n\t"
    "ds_read_b128 v[20:23], v114 offset:11264\n\t"
    "ds_read_b128 v[24:27], v114 offset:19456\n\t"
    "ds_read_b128 v[28:31], v114 offset:27648\n\t"
    "s_waitcnt lgkmcnt(7)\n\t"
    "v_mfma_f32_16x16x32_bf16 v[48:51], a[16:19], v[0:3], v[48:51]\n\t"
    "s_waitcnt lgkmcnt(6)\n\t"
    "v_mfma_f32_16x16x32_bf16 v[48:51], a[48:51], v[4:7], v[48:51]\n\t"
    "s_waitcnt lgkmcnt(5)\n\t"
    "v_mfma_f32_16x16x32_bf16 v[48:51], a[80:83], v[8:11], v[48:51]\n\t"
    "s_waitcnt lgkmcnt(4)\n\t"
    "v_mfma_f32_16x16x32_bf16 v[48:51], a[112:115], v[12:15], v[48:51]\n\t"
    "v_mfma_f32_16x16x32_bf16 v[56:59], a[20:23], v[0:3], v[56:59]\n\t"
    "v_mfma_f32_16x16x32_bf16 v[56:59], a[52:55], v[4:7], v[56:59]\n\t"
    "v_mfma_f32_16x16x32_bf16 v[56:59], a[84:87], v[8:11], v[56:59]\n\t"
    "v_mfma_f32_16x16x32_bf16 v[56:59], a[116:119], v[12:15], v[56:59]\n\t"
    "ds_read_b128 v[0:3], v114 offset:4096\n\t"
    "ds_read_b128 v[4:7], v114 offset:12288\n\t"
    "ds_read_b128 v[8:11], v114 offset:20480\n\t"
    "ds_read_b128 v[12:15], v114 offset:28672\n\t"
    "s_waitcnt lgkmcnt(7)\n\t"
    "v_mfma_f32_16x16x32_bf16 v[52:55], a[16:19], v[16:19], v[52:55]\n\t"
    "s_waitcnt lgkmcnt(6)\n\t"
    "v_mfma_f32_16x16x32_bf16 v[52:55], a[48:51], v[20:23], v[52:55]\n\t"
    "s_waitcnt lgkmcnt(5)\n\t"
    "v_mfma_f32_16x16x32_bf16 v[52:55], a[80:83], v[24:27], v[52:55]\n\t"
    "s_waitcnt lgkmcnt(4)\n\t"
    "v_mfma_f32_16x16x32_bf16 v[52:55], a[112:115], v[28:31], v[52:55]\n\t"
    "v_mfma_f32_16x16x32_bf16 v[60:63], a[20:23], v[16:19], v[60:63]\n\t"
    "v_mfma_f32_16x16x32_bf16 v[60:63], a[52:55], v[20:23], v[60:63]\n\t"
    "v_mfma_f32_16x16x32_bf16 v[60:63], a[84:87], v[24:27], v[60:63]\n\t"
    "v_mfma_f32_16x16x32_bf16 v[60:63], a[116:119], v[28:31], v[60:63]\n\t"
    "ds_read_b128 v[16:19], v114 offset:5120\n\t"
    "ds_read_b128 v[20:23], v114 offset:13312\n\t"
    "ds_read_b128 v[24:27], v114 offset:21504\n\t"
    "ds_read_b128 v[28:31], v114 offset:29696\n\t"
    "s_waitcnt lgkmcnt(7)\n\t"
    "v_mfma_f32_16x16x32_bf16 v[64:67], a[16:19], v[0:3], v[64:67]\n\t"
    "s_waitcnt lgkmcnt(6)\n\t"
    "v_mfma_f32_16x16x32_bf16 v[64:67], a[48:51], v[4:7], v[64:67]\n\t"
    "s_waitcnt lgkmcnt(5)\n\t"
    "v_mfma_f32_16x16x32_bf16 v[64:67], a[80:83], v[8:11], v[64:67]\n\t"
    "s_waitcnt lgkmcnt(4)\n\t"
    "v_mfma_f32_16x16x32_bf16 v[64:67], a[112:115], v[12:15], v[64:67]\n\t"
    "v_mfma_f32_16x16x32_bf16 v[72:75], a[20:23], v[0:3], v[72:75]\n\t"
    "v_mfma_f32_16x16x32_bf16 v[72:75], a[52:55], v[4:7], v[72:75]\n\t"
    "v_mfma_f32_16x16x32_bf16 v[72:75], a[84:87], v[8:11], v[72:75]\n\t"
    "v_mfma_f32_16x16x32_bf16 v[72:75], a[116:119], v[12:15], v[72:75]\n\t"
    "ds_read_b128 v[0:3], v114 offset:6144\n\t"
    "ds_read_b128 v[4:7], v114 offset:14336\n\t"
    "ds_read_b128 v[8:11], v114 offset:22528\n\t"
    "ds_read_b128 v[12:15], v114 offset:30720\n\t"
    "s_waitcnt lgkmcnt(7)\n\t"
    "v_mfma_f32_16x16x32_bf16 v[68:71], a[16:19], v[16:19], v[68:71]\n\t"
    "s_waitcnt lgkmcnt(6)\n\t"
    "v_mfma_f32_16x16x32_bf16 v[68:71], a[48:51], v[20:23], v[68:71]\n\t"
    "s_waitcnt lgkmcnt(5)\n\t"
    "v_mfma_f32_16x16x32_bf16 v[68:71], a[80:83], v[24:27], v[68:71]\n\t"
    "s_waitcnt lgkmcnt(4)\n\t"
    "v_mfma_f32_16x16x32_bf16 v[68:71], a[112:115], v[28:31], v[68:71]\n\t"
    "v_mfma_f32_16x16x32_bf16 v[76:79], a[20:23], v[16:19], v[76:79]\n\t"
    "v_mfma_f32_16x16x32_bf16 v[76:79], a[52:55], v[20:23], v[76:79]\n\t"
    "v_mfma_f32_16x16x32_bf16 v[76:79], a[84:87], v[24:27], v[76:79]\n\t"
    "v_mfma_f32_16x16x32_bf16 v[76:79], a[116:119], v[28:31], v[76:79]\n\t"
    "ds_read_b128 v[16:19], v114 offset:7168\n\t"
    "ds_read_b128 v[20:23], v114 offset:15360\n\t"
    "ds_read_b128 v[24:27], v114 offset:23552\n\t"
    "ds_read_b128 v[28:31], v114 offset:31744\n\t"
    "s_waitcnt lgkmcnt(7)\n\t"
    "v_mfma_f32_16x16x32_bf16 v[80:83], a[16:19], v[0:3], v[80:83]\n\t"
    "s_waitcnt lgkmcnt(6)\n\t"
    "v_mfma_f32_16x16x32_bf16 v[80:83], a[48:51], v[4:7], v[80:83]\n\t"
    "s_waitcnt lgkmcnt(5)\n\t"
    "v_mfma_f32_16x16x32_bf16 v[80:83], a[80:83], v[8:11], v[80:83]\n\t"
    "s_waitcnt lgkmcnt(4)\n\t"
    "v_mfma_f32_16x16x32_bf16 v[80:83], a[112:115], v[12:15], v[80:83]\n\t"
    "v_mfma_f32_16x16x32_bf16 v[88:91], a[20:23], v[0:3], v[88:91]\n\t"
    "v_mfma_f32_16x16x32_bf16 v[88:91], a[52:55], v[4:7], v[88:91]\n\t"
    "v_mfma_f32_16x16x32_bf16 v[88:91], a[84:87], v[8:11], v[88:91]\n\t"
    "v_mfma_f32_16x16x32_bf16 v[88:91], a[116:119], v[12:15], v[88:91]\n\t"
    "s_waitcnt lgkmcnt(3)\n\t"
    "v_mfma_f32_16x16x32_bf16 v[84:87], a[16:19], v[16:19], v[84:87]\n\t"
    "s_waitcnt lgkmcnt(2)\n\t"
    "v_mfma_f32_16x16x32_bf16 v[84:87], a[48:51], v[20:23], v[84:87]\n\t"
    "s_waitcnt lgkmcnt(1)\n\t"
    "v_mfma_f32_16x16x32_bf16 v[84:87], a[80:83], v[24:27], v[84:87]\n\t"
    "s_waitcnt lgkmcnt(0)\n\t"
    "v_mfma_f32_16x16x32_bf16 v[84:87], a[112:115], v[28:31], v[84:87]\n\t"
    "v_mfma_f32_16x16x32_bf16 v[92:95], a[20:23], v[16:19], v[92:95]\n\t"
    "v_mfma_f32_16x16x32_bf16 v[92:95], a[52:55], v[20:23], v[92:95]\n\t"
    "v_mfma_f32_16x16x32_bf16 v[92:95], a[84:87], v[24:27], v[92:95]\n\t"
    "v_mfma_f32_16x16x32_bf16 v[92:95], a[116:119], v[28:31], v[92:95]\n\t"
    "s_waitcnt vmcnt(11)\n\t"
    "ds_read_b64_tr_b16 a[24:25], v97 offset:4096\n\t"
    "ds_read_b64_tr_b16 a[28:29], v98 offset:4096\n\t"
    "ds_read_b128 v[104:107], v99 offset:4096\n\t"
    "s_waitcnt lgkmcnt(0)\n\t"
    "v_and_b32 v104, 0x7fff7fff, v104\n\t"
    "v_pk_max_u16 v103, v103, v104\n\t"
    "v_and_b32 v105, 0x7fff7fff, v105\n\t"
    "v_pk_max_u16 v103, v103, v105\n\t"
    "v_and_b32 v106, 0x7fff7fff, v106\n\t"
    "v_pk_max_u16 v103, v103, v106\n\t"
    "v_and_b32 v107, 0x7fff7fff, v107\n\t"
    "v_pk_max_u16 v103, v103, v107\n\t"
    "s_waitcnt vmcnt(10)\n\t"
    "ds_read_b64_tr_b16 a[26:27], v97 offset:5120\n\t"
    "ds_read_b64_tr_b16 a[30:31], v98 offset:5120\n\t"
    "ds_read_b128 v[104:107], v99 offset:5120\n\t"
    "s_waitcnt lgkmcnt(0)\n\t"
    "v_and_b32 v104, 0x7fff7fff, v104\n\t"
    "v_pk_max_u16 v103, v103, v104\n\t"
    "v_and_b32 v105, 0x7fff7fff, v105\n\t"
    "v_pk_max_u16 v103, v103, v105\n\t"
    "v_and_b32 v106, 0x7fff7fff, v106\n\t"
    "v_pk_max_u16 v103, v103, v106\n\t"
    "v_and_b32 v107, 0x7fff7fff, v107\n\t"
    "v_pk_max_u16 v103, v103, v107\n\t"
    "s_waitcnt vmcnt(9)\n\t"
    "ds_read_b64_tr_b16 a[56:57], v97 offset:6144\n\t"
    "ds_read_b64_tr_b16 a[60:61], v98 offset:6144\n\t"
    "ds_read_b128 v[104:107], v99 offset:6144\n\t"
    "s_waitcnt lgkmcnt(0)\n\t"
    "v_and_b32 v104, 0x7fff7fff, v104\n\t"
    "v_pk_max_u16 v103, v103, v104\n\t"
    "v_and_b32 v105, 0x7fff7fff, v105\n\t"
    "v_pk_max_u16 v103, v103, v105\n\t"
    "v_and_b32 v106, 0x7fff7fff, v106\n\t"
    "v_pk_max_u16 v103, v103, v106\n\t"
    "v_and_b32 v107, 0x7fff7fff, v107\n\t"
    "v_pk_max_u16 v103, v103, v107\n\t"
    "s_waitcnt vmcnt(8)\n\t"
    "ds_read_b64_tr_b16 a[58:59], v97 offset:7168\n\t"
    "ds_read_b64_tr_b16 a[62:63], v98 offset:7168\n\t"
    "ds_read_b128 v[104:107], v99 offset:7168\n\t"
    "s_waitcnt lgkmcnt(0)\n\t"
    "v_and_b32 v104, 0x7fff7fff, v104\n\t"
    "v_pk_max_u16 v103, v103, v104\n\t"
    "v_and_b32 v105, 0x7fff7fff, v105\n\t"
    "v_pk_max_u16 v103, v103, v105\n\t"
    "v_and_b32 v106, 0x7fff7fff, v106\n\t"
    "v_pk_max_u16 v103, v103, v106\n\t"
    "v_and_b32 v107, 0x7fff7fff, v107\n\t"
    "v_pk_max_u16 v103, v103, v107\n\t"
    "s_waitcnt vmcnt(7)\n\t"
    "ds_read_b64_tr_b16 a[88:89], v97 offset:8192\n\t"
    "ds_read_b64_tr_b16 a[92:93], v98 offset:8192\n\t"
    "ds_read_b128 v[104:107], v99 offset:8192\n\t"
    "s_waitcnt lgkmcnt(0)\n\t"
    "v_and_b32 v104, 0x7fff7fff, v104\n\t"
    "v_pk_max_u16 v103, v103, v104\n\t"
    "v_and_b32 v105, 0x7fff7fff, v105\n\t"
    "v_pk_max_u16 v103, v103, v105\n\t"
    "v_and_b32 v106, 0x7fff7fff, v106\n\t"
    "v_pk_max_u16 v103, v103, v106\n\t"
    "v_and_b32 v107, 0x7fff7fff, v107\n\t"
    "v_pk_max_u16 v103, v103, v107\n\t"
    "s_waitcnt vmcnt(6)\n\t"
    "ds_read_b64_tr_b16 a[90:91], v97 offset:9216\n\t"
    "ds_read_b64_tr_b16 a[94:95], v98 offset:9216\n\t"
    "ds_read_b128 v[104:107], v99 offset:9216\n\t"
    "s_waitcnt lgkmcnt(0)\n\t"
    "v_and_b32 v104, 0x7fff7fff, v104\n\t"
    "v_pk_max_u16 v103, v103, v104\n\t"
    "v_and_b32 v105, 0x7fff7fff, v105\n\t"
    "v_pk_max_u16 v103, v103, v105\n\t"
    "v_and_b32 v106, 0x7fff7fff, v106\n\t"
    "v_pk_max_u16 v103, v103, v106\n\t"
    "v_and_b32 v107, 0x7fff7fff, v107\n\t"
    "v_pk_max_u16 v103, v103, v107\n\t"
    "s_waitcnt vmcnt(5)\n\t"
    "ds_read_b64_tr_b16 a[120:121], v97 offset:0\n\t"
    "ds_read_b64_tr_b16 a[124:125], v98 offset:0\n\t"
    "ds_read_b128 v[104:107], v99 offset:0\n\t"
    "s_waitcnt lgkmcnt(0)\n\t"
    "v_and_b32 v104, 0x7fff7fff, v104\n\t"
    "v_pk_max_u16 v103, v103, v104\n\t"
    "v_and_b32 v105, 0x7fff7fff, v105\n\t"
    "v_pk_max_u16 v103, v103, v105\n\t"
    "v_and_b32 v106, 0x7fff7fff, v106\n\t"
    "v_pk_max_u16 v103, v103, v106\n\t"
    "v_and_b32 v107, 0x7fff7fff, v107\n\t"
    "v_pk_max_u16 v103, v103, v107\n\t"
    "s_waitcnt vmcnt(4)\n\t"
    "ds_read_b64_tr_b16 a[122:123], v97 offset:1024\n\t"
    "ds_read_b64_tr_b16 a[126:127], v98 offset:1024\n\t"
    "ds_read_b128 v[104:107], v99 offset:1024\n\t"
    "s_waitcnt lgkmcnt(0)\n\t"
    "v_and_b32 v104, 0x7fff7fff, v104\n\t"
    "v_pk_max_u16 v103, v103, v104\n\t"
    "v_and_b32 v105, 0x7fff7fff, v105\n\t"
    "v_pk_max_u16 v103, v103, v105\n\t"
    "v_and_b32 v106, 0x7fff7fff, v106\n\t"
    "v_pk_max_u16 v103, v103, v106\n\t"
    "v_and_b32 v107, 0x7fff7fff, v107\n\t"
    "v_pk_max_u16 v103, v103, v107\n\t"
    "v_and_b32 v108, 0xffff, v103\n\t"
    "v_lshrrev_b32 v103, 16, v103\n\t"
    "v_max_u32 v103, v103, v108\n\t"
    "ds_max_u32 v101, v103\n\t"
    "ds_read_b32 v103, v101\n\t"
    "s_waitcnt lgkmcnt(0)\n\t"
    "v_lshlrev_b32 v103, 16, v103\n\t"
    "v_rcp_f32 v103, v103\n\t"
    "s_nop 0\n\t"
    "v_mul_f32 v122, 0x7e800000, v103\n\t"
    "v_min_f32 v122, 0x71800000, v122\n\t"
    "s_waitcnt vmcnt(0)\n\t"
    "s_barrier\n\t"
    "s_setprio 1\n\t"
    "s_add_u32 m0, s65, s53\n\t"
    "s_nop 0\n\t"
    "buffer_load_dwordx4 v120, s[40:43], s54 offen lds\n\t"
    "s_add_u32 m0, m0, 0x400\n\t"
    "s_add_u32 s56, s54, 128\n\t"
    "buffer_load_dwordx4 v120, s[40:43], s56 offen lds\n\t"
    "s_add_u32 s54, s54, 0x2000\n\t"
    "s_add_u32 s53, s53, 0x2000\n\t"
    "s_and_b32 s53, s53, 0xffff\n\t"
    "ds_read_b128 v[0:3], v114 offset:32768\n\t"
    "ds_read_b128 v[4:7], v114 offset:40960\n\t"
    "ds_read_b128 v[8:11], v114 offset:49152\n\t"
    "ds_read_b128 v[12:15], v114 offset:57344\n\t"
    "ds_read_b128 v[16:19], v114 offset:33792\n\t"
    "ds_read_b128 v[20:23], v114 offset:41984\n\t"
    "ds_read_b128 v[24:27], v114 offset:50176\n\t"
    "ds_read_b128 v[28:31], v114 offset:58368\n\t"
    "ds_read_b32 v124, v112 offset:0\n\t"
    "ds_read_b32 v125, v112 offset:128\n\t"
    "ds_read_b32 v126, v112 offset:256\n\t"
    "ds_read_b32 v127, v112 offset:384\n\t"
    "s_waitcnt lgkmcnt(2)\n\t"
    "v_add_f32 v108, v124, v125\n\t"
    "s_waitcnt lgkmcnt(0)\n\t"
    "v_add_f32 v109, v126, v127\n\t"
    "v_add_f32 v108, v108, v109\n\t"
    "v_cmp_nlt_f32 vcc, s64, v108\n\t"
    "v_cmp_nlt_f32_e64 s[62:63], v108, v122\n\t"
    "v_rcp_f32 v104, v108\n\t"
    "s_nop 0\n\t"
    "v_mov_b32 v105, v104\n\t"
    "s_nop 1\n\t"
    "v_permlane32_swap_b32 v104, v105\n\t"
    "s_or_b64 s[62:63], s[62:63], vcc\n\t"
    "s_cmp_lg_u64 s[62:63], 0\n\t"
    "s_cselect_b64 s[62:63], s[60:61], 0\n\t"
    "s_or_b64 s[58:59], s[58:59], s[62:63]\n\t"
    "s_lshl_b64 s[60:61], s[60:61], 1\n\t"
    "ds_read_b32 v124, v112 offset:512\n\t"
    "ds_read_b32 v125, v112 offset:640\n\t"
    "ds_read_b32 v126, v112 offset:768\n\t"
    "ds_read_b32 v127, v112 offset:896\n\t"
    "s_waitcnt lgkmcnt(2)\n\t"
    "v_add_f32 v108, v124, v125\n\t"
    "s_waitcnt lgkmcnt(0)\n\t"
    "v_add_f32 v109, v126, v127\n\t"
    "v_add_f32 v108, v108, v109\n\t"
    "v_cmp_nlt_f32 vcc, s64, v108\n\t"
    "v_cmp_nlt_f32_e64 s[62:63], v108, v122\n\t"
    "v_rcp_f32 v106, v108\n\t"
    "s_nop 0\n\t"
    "v_mov_b32 v107, v106\n\t"
    "s_nop 1\n\t"
    "v_permlane32_swap_b32 v106, v107\n\t"
    "s_or_b64 s[62:63], s[62:63], vcc\n\t"
    "s_cmp_lg_u64 s[62:63], 0\n\t"
    "s_cselect_b64 s[62:63], s[60:61], 0\n\t"
    "s_or_b64 s[58:59], s[58:59], s[62:63]\n\t"
    "s_lshl_b64 s[60:61], s[60:61], 1\n\t"
    "ds_read_b32 v124, v112 offset:1024\n\t"
    "ds_read_b32 v125, v112 offset:1152\n\t"
    "ds_read_b32 v126, v112 offset:1280\n\t"
    "ds_read_b32 v127, v112 offset:1408\n\t"
    "s_waitcnt lgkmcnt(2)\n\t"
    "v_add_f32 v108, v124, v125\n\t"
    "s_waitcnt lgkmcnt(0)\n\t"
    "v_add_f32 v109, v126, v127\n\t"
    "v_add_f32 v108, v108, v109\n\t"
    "v_cmp_nlt_f32 vcc, s64, v108\n\t"
    "v_cmp_nlt_f32_e64 s[62:63], v108, v122\n\t"
    "v_rcp_f32 v110, v108\n\t"
    "s_nop 0\n\t"
    "v_mov_b32 v111, v110\n\t"
    "s_nop 1\n\t"
    "v_permlane32_swap_b32 v110, v111\n\t"
    "s_or_b64 s[62:63], s[62:63], vcc\n\t"
    "s_cmp_lg_u64 s[62:63], 0\n\t"
    "s_cselect_b64 s[62:63], s[60:61], 0\n\t"
    "s_or_b64 s[58:59], s[58:59], s[62:63]\n\t"
    "s_lshl_b64 s[60:61], s[60:61], 1\n\t"
    "ds_read_b32 v124, v112 offset:1536\n\t"
    "ds_read_b32 v125, v112 offset:1664\n\t"
    "ds_read_b32 v126, v112 offset:1792\n\t"
    "ds_read_b32 v127, v112 offset:1920\n\t"
    "s_waitcnt lgkmcnt(2)\n\t"
    "v_add_f32 v108, v124, v125\n\t"
    "s_waitcnt lgkmcnt(0)\n\t"
    "v_add_f32 v109, v126, v127\n\t"
    "v_add_f32 v108, v108, v109\n\t"
    "v_cmp_nlt_f32 vcc, s64, v108\n\t"
    "v_cmp_nlt_f32_e64 s[62:63], v108, v122\n\t"
    "v_rcp_f32 v116, v108\n\t"
    "s_nop 0\n\t"
    "v_mov_b32 v117, v116\n\t"
    "s_nop 1\n\t"
    "v_permlane32_swap_b32 v116, v117\n\t"
    "s_or_b64 s[62:63], s[62:63], vcc\n\t"
    "s_cmp_lg_u64 s[62:63], 0\n\t"
    "s_cselect_b64 s[62:63], s[60:61], 0\n\t"
    "s_or_b64 s[58:59], s[58:59], s[62:63]\n\t"
    "s_lshl_b64 s[60:61], s[60:61], 1\n\t"
    "v_mfma_f32_16x16x32_bf16 v[32:35], a[24:27], v[0:3], v[32:35]\n\t"
    "v_mfma_f32_16x16x32_bf16 v[32:35], a[56:59], v[4:7], v[32:35]\n\t"
    "v_mfma_f32_16x16x32_bf16 v[32:35], a[88:91], v[8:11], v[32:35]\n\t"
    "v_mfma_f32_16x16x32_bf16 v[32:35], a[120:123], v[12:15], v[32:35]\n\t"
    "v_mfma_f32_16x16x32_bf16 v[40:43], a[28:31], v[0:3], v[40:43]\n\t"
    "v_mfma_f32_16x16x32_bf16 v[40:43], a[60:63], v[4:7], v[40:43]\n\t"
    "v_mfma_f32_16x16x32_bf16 v[40:43], a[92:95], v[8:11], v[40:43]\n\t"
    "v_mfma_f32_16x16x32_bf16 v[40:43], a[124:127], v[12:15], v[40:43]\n\t"
    "ds_read_b128 v[0:3], v114 offset:34816\n\t"
    "ds_read_b128 v[4:7], v114 offset:43008\n\t"
    "ds_read_b128 v[8:11], v114 offset:51200\n\t"
    "ds_read_b128 v[12:15], v114 offset:59392\n\t"
    "v_mfma_f32_16x16x32_bf16 v[36:39], a[24:27], v[16:19], v[36:39]\n\t"
    "s_setprio 2\n\t"
    "v_mfma_f32_16x16x32_bf16 v[36:39], a[56:59], v[20:23], v[36:39]\n\t"
    "v_mul_f32 v32, v32, v104\n\t"
    "v_mul_f32 v33, v33, v104\n\t"
    "v_mfma_f32_16x16x32_bf16 v[36:39], a[88:91], v[24:27], v[36:39]\n\t"
    "v_mul_f32 v34, v34, v104\n\t"
    "v_mul_f32 v35, v35, v104\n\t"
    "v_mfma_f32_16x16x32_bf16 v[36:39], a[120:123], v[28:31], v[36:39]\n\t"
    "v_cvt_pk_bf16_f32 v96, v32, v33\n\t"
    "v_cvt_pk_bf16_f32 v97, v34, v35\n\t"
    "v_mfma_f32_16x16x32_bf16 v[44:47], a[28:31], v[16:19], v[44:47]\n\t"
    "v_mul_f32 v40, v40, v104\n\t"
    "v_mul_f32 v41, v41, v104\n\t"
    "v_mfma_f32_16x16x32_bf16 v[44:47], a[60:63], v[20:23], v[44:47]\n\t"
    "v_mul_f32 v42, v42, v104\n\t"
    "v_mul_f32 v43, v43, v104\n\t"
    "v_mfma_f32_16x16x32_bf16 v[44:47], a[92:95], v[24:27], v[44:47]\n\t"
    "v_cvt_pk_bf16_f32 v98, v40, v41\n\t"
    "v_cvt_pk_bf16_f32 v99, v42, v43\n\t"
    "v_mfma_f32_16x16x32_bf16 v[44:47], a[124:127], v[28:31], v[44:47]\n\t"
    "v_permlane16_swap_b32 v96, v98\n\t"
    "v_permlane16_swap_b32 v97, v99\n\t"
    "s_setprio 1\n\t"
    "s_add_u32 s57, s55, 0\n\t"
    "buffer_store_dwordx4 v[96:99], v123, s[48:51], s57 offen sc1\n\t"
    "ds_read_b128 v[16:19], v114 offset:35840\n\t"
    "ds_read_b128 v[20:23], v114 offset:44032\n\t"
    "ds_read_b128 v[24:27], v114 offset:52224\n\t"
    "ds_read_b128 v[28:31], v114 offset:60416\n\t"
    "s_waitcnt lgkmcnt(7)\n\t"
    "v_mfma_f32_16x16x32_bf16 v[48:51], a[24:27], v[0:3], v[48:51]\n\t"
    "s_setprio 2\n\t"
    "s_waitcnt lgkmcnt(6)\n\t"
    "v_mfma_f32_16x16x32_bf16 v[48:51], a[56:59], v[4:7], v[48:51]\n\t"
    "v_mul_f32 v36, v36, v105\n\t"
    "v_mul_f32 v37, v37, v105\n\t"
    "s_waitcnt lgkmcnt(5)\n\t"
    "v_mfma_f32_16x16x32_bf16 v[48:51], a[88:91], v[8:11], v[48:51]\n\t"
    "v_mul_f32 v38, v38, v105\n\t"
    "v_mul_f32 v39, v39, v105\n\t"
    "s_waitcnt lgkmcnt(4)\n\t"
    "v_mfma_f32_16x16x32_bf16 v[48:51], a[120:123], v[12:15], v[48:51]\n\t"
    "v_cvt_pk_bf16_f32 v100, v36, v37\n\t"
    "v_cvt_pk_bf16_f32 v101, v38, v39\n\t"
    "v_mfma_f32_16x16x32_bf16 v[56:59], a[28:31], v[0:3], v[56:59]\n\t"
    "v_mul_f32 v44, v44, v105\n\t"
    "v_mul_f32 v45, v45, v105\n\t"
    "v_mfma_f32_16x16x32_bf16 v[56:59], a[60:63], v[4:7], v[56:59]\n\t"
    "v_mul_f32 v46, v46, v105\n\t"
    "v_mul_f32 v47, v47, v105\n\t"
    "v_mfma_f32_16x16x32_bf16 v[56:59], a[92:95], v[8:11], v[56:59]\n\t"
    "v_cvt_pk_bf16_f32 v102, v44, v45\n\t"
    "v_cvt_pk_bf16_f32 v103, v46, v47\n\t"
    "v_mfma_f32_16x16x32_bf16 v[56:59], a[124:127], v[12:15], v[56:59]\n\t"
    "v_permlane16_swap_b32 v100, v102\n\t"
    "v_permlane16_swap_b32 v101, v103\n\t"
    "s_setprio 1\n\t"
    "s_add_u32 s57, s55, 4096\n\t"
    "buffer_store_dwordx4 v[100:103], v123, s[48:51], s57 offen sc1\n\t"
    "ds_read_b128 v[0:3], v114 offset:36864\n\t"
    "ds_read_b128 v[4:7], v114 offset:45056\n\t"
    "ds_read_b128 v[8:11], v114 offset:53248\n\t"
    "ds_read_b128 v[12:15], v114 offset:61440\n\t"
    "s_waitcnt lgkmcnt(7)\n\t"
    "v_mfma_f32_16x16x32_bf16 v[52:55], a[24:27], v[16:19], v[52:55]\n\t"
    "s_setprio 2\n\t"
    "s_waitcnt lgkmcnt(6)\n\t"
    "v_mfma_f32_16x16x32_bf16 v[52:55], a[56:59], v[20:23], v[52:55]\n\t"
    "v_mul_f32 v48, v48, v106\n\t"
    "v_mul_f32 v49, v49, v106\n\t"
    "s_waitcnt lgkmcnt(5)\n\t"
    "v_mfma_f32_16x16x32_bf16 v[52:55], a[88:91], v[24:27], v[52:55]\n\t"
    "v_mul_f32 v50, v50, v106\n\t"
    "v_mul_f32 v51, v51, v106\n\t"
    "s_waitcnt lgkmcnt(4)\n\t"
    "v_mfma_f32_16x16x32_bf16 v[52:55], a[120:123], v[28:31], v[52:55]\n\t"
    "v_cvt_pk_bf16_f32 v96, v48, v49\n\t"
    "v_cvt_pk_bf16_f32 v97, v50, v51\n\t"
    "v_mfma_f32_16x16x32_bf16 v[60:63], a[28:31], v[16:19], v[60:63]\n\t"
    "v_mul_f32 v56, v56, v106\n\t"
    "v_mul_f32 v57, v57, v106\n\t"
    "v_mfma_f32_16x16x32_bf16 v[60:63], a[60:63], v[20:23], v[60:63]\n\t"
    "v_mul_f32 v58, v58, v106\n\t"
    "v_mul_f32 v59, v59, v106\n\t"
    "v_mfma_f32_16x16x32_bf16 v[60:63], a[92:95], v[24:27], v[60:63]\n\t"
    "v_cvt_pk_bf16_f32 v98, v56, v57\n\t"
    "v_cvt_pk_bf16_f32 v99, v58, v59\n\t"
    "v_mfma_f32_16x16x32_bf16 v[60:63], a[124:127], v[28:31], v[60:63]\n\t"
    "v_permlane16_swap_b32 v96, v98\n\t"
    "v_permlane16_swap_b32 v97, v99\n\t"
    "s_setprio 1\n\t"
    "s_add_u32 s57, s55, 8192\n\t"
    "buffer_store_dwordx4 v[96:99], v123, s[48:51], s57 offen sc1\n\t"
    "ds_read_b128 v[16:19], v114 offset:37888\n\t"
    "ds_read_b128 v[20:23], v114 offset:46080\n\t"
    "ds_read_b128 v[24:27], v114 offset:54272\n\t"
    "ds_read_b128 v[28:31], v114 offset:62464\n\t"
    "s_waitcnt lgkmcnt(7)\n\t"
    "v_mfma_f32_16x16x32_bf16 v[64:67], a[24:27], v[0:3], v[64:67]\n\t"
    "s_setprio 2\n\t"
    "s_waitcnt lgkmcnt(6)\n\t"
    "v_mfma_f32_16x16x32_bf16 v[64:67], a[56:59], v[4:7], v[64:67]\n\t"
    "v_mul_f32 v52, v52, v107\n\t"
    "v_mul_f32 v53, v53, v107\n\t"
    "s_waitcnt lgkmcnt(5)\n\t"
    "v_mfma_f32_16x16x32_bf16 v[64:67], a[88:91], v[8:11], v[64:67]\n\t"
    "v_mul_f32 v54, v54, v107\n\t"
    "v_mul_f32 v55, v55, v107\n\t"
    "s_waitcnt lgkmcnt(4)\n\t"
    "v_mfma_f32_16x16x32_bf16 v[64:67], a[120:123], v[12:15], v[64:67]\n\t"
    "v_cvt_pk_bf16_f32 v100, v52, v53\n\t"
    "v_cvt_pk_bf16_f32 v101, v54, v55\n\t"
    "v_mfma_f32_16x16x32_bf16 v[72:75], a[28:31], v[0:3], v[72:75]\n\t"
    "v_mul_f32 v60, v60, v107\n\t"
    "v_mul_f32 v61, v61, v107\n\t"
    "v_mfma_f32_16x16x32_bf16 v[72:75], a[60:63], v[4:7], v[72:75]\n\t"
    "v_mul_f32 v62, v62, v107\n\t"
    "v_mul_f32 v63, v63, v107\n\t"
    "v_mfma_f32_16x16x32_bf16 v[72:75], a[92:95], v[8:11], v[72:75]\n\t"
    "v_cvt_pk_bf16_f32 v102, v60, v61\n\t"
    "v_cvt_pk_bf16_f32 v103, v62, v63\n\t"
    "v_mfma_f32_16x16x32_bf16 v[72:75], a[124:127], v[12:15], v[72:75]\n\t"
    "v_permlane16_swap_b32 v100, v102\n\t"
    "v_permlane16_swap_b32 v101, v103\n\t"
    "s_setprio 1\n\t"
    "s_add_u32 s57, s55, 12288\n\t"
    "buffer_store_dwordx4 v[100:103], v123, s[48:51], s57 offen sc1\n\t"
    "ds_read_b128 v[0:3], v114 offset:38912\n\t"
    "ds_read_b128 v[4:7], v114 offset:47104\n\t"
    "ds_read_b128 v[8:11], v114 offset:55296\n\t"
    "ds_read_b128 v[12:15], v114 offset:63488\n\t"
    "s_waitcnt lgkmcnt(7)\n\t"
    "v_mfma_f32_16x16x32_bf16 v[68:71], a[24:27], v[16:19], v[68:71]\n\t"
    "s_setprio 2\n\t"
    "s_waitcnt lgkmcnt(6)\n\t"
    "v_mfma_f32_16x16x32_bf16 v[68:71], a[56:59], v[20:23], v[68:71]\n\t"
    "v_mul_f32 v64, v64, v110\n\t"
    "v_mul_f32 v65, v65, v110\n\t"
    "s_waitcnt lgkmcnt(5)\n\t"
    "v_mfma_f32_16x16x32_bf16 v[68:71], a[88:91], v[24:27], v[68:71]\n\t"
    "v_mul_f32 v66, v66, v110\n\t"
    "v_mul_f32 v67, v67, v110\n\t"
    "s_waitcnt lgkmcnt(4)\n\t"
    "v_mfma_f32_16x16x32_bf16 v[68:71], a[120:123], v[28:31], v[68:71]\n\t"
    "v_cvt_pk_bf16_f32 v96, v64, v65\n\t"
    "v_cvt_pk_bf16_f32 v97, v66, v67\n\t"
    "v_mfma_f32_16x16x32_bf16 v[76:79], a[28:31], v[16:19], v[76:79]\n\t"
    "v_mul_f32 v72, v72, v110\n\t"
    "v_mul_f32 v73, v73, v110\n\t"
    "v_mfma_f32_16x16x32_bf16 v[76:79], a[60:63], v[20:23], v[76:79]\n\t"
    "v_mul_f32 v74, v74, v110\n\t"
    "v_mul_f32 v75, v75, v110\n\t"
    "v_mfma_f32_16x16x32_bf16 v[76:79], a[92:95], v[24:27], v[76:79]\n\t"
    "v_cvt_pk_bf16_f32 v98, v72, v73\n\t"
    "v_cvt_pk_bf16_f32 v99, v74, v75\n\t"
    "v_mfma_f32_16x16x32_bf16 v[76:79], a[124:127], v[28:31], v[76:79]\n\t"
    "v_permlane16_swap_b32 v96, v98\n\t"
    "v_permlane16_swap_b32 v97, v99\n\t"
    "s_setprio 1\n\t"
    "s_add_u32 s57, s55, 16384\n\t"
    "buffer_store_dwordx4 v[96:99], v123, s[48:51], s57 offen sc1\n\t"
    "ds_read_b128 v[16:19], v114 offset:39936\n\t"
    "ds_read_b128 v[20:23], v114 offset:48128\n\t"
    "ds_read_b128 v[24:27], v114 offset:56320\n\t"
    "ds_read_b128 v[28:31], v114 offset:64512\n\t"
    "s_waitcnt lgkmcnt(7)\n\t"
    "v_mfma_f32_16x16x32_bf16 v[80:83], a[24:27], v[0:3], v[80:83]\n\t"
    "s_setprio 2\n\t"
    "s_waitcnt lgkmcnt(6)\n\t"
    "v_mfma_f32_16x16x32_bf16 v[80:83], a[56:59], v[4:7], v[80:83]\n\t"
    "v_mul_f32 v68, v68, v111\n\t"
    "v_mul_f32 v69, v69, v111\n\t"
    "s_waitcnt lgkmcnt(5)\n\t"
    "v_mfma_f32_16x16x32_bf16 v[80:83], a[88:91], v[8:11], v[80:83]\n\t"
    "v_mul_f32 v70, v70, v111\n\t"
    "v_mul_f32 v71, v71, v111\n\t"
    "s_waitcnt lgkmcnt(4)\n\t"
    "v_mfma_f32_16x16x32_bf16 v[80:83], a[120:123], v[12:15], v[80:83]\n\t"
    "v_cvt_pk_bf16_f32 v100, v68, v69\n\t"
    "v_cvt_pk_bf16_f32 v101, v70, v71\n\t"
    "v_mfma_f32_16x16x32_bf16 v[88:91], a[28:31], v[0:3], v[88:91]\n\t"
    "v_mul_f32 v76, v76, v111\n\t"
    "v_mul_f32 v77, v77, v111\n\t"
    "v_mfma_f32_16x16x32_bf16 v[88:91], a[60:63], v[4:7], v[88:91]\n\t"
    "v_mul_f32 v78, v78, v111\n\t"
    "v_mul_f32 v79, v79, v111\n\t"
    "v_mfma_f32_16x16x32_bf16 v[88:91], a[92:95], v[8:11], v[88:91]\n\t"
    "v_cvt_pk_bf16_f32 v102, v76, v77\n\t"
    "v_cvt_pk_bf16_f32 v103, v78, v79\n\t"
    "v_mfma_f32_16x16x32_bf16 v[88:91], a[124:127], v[12:15], v[88:91]\n\t"
    "v_permlane16_swap_b32 v100, v102\n\t"
    "v_permlane16_swap_b32 v101, v103\n\t"
    "s_setprio 1\n\t"
    "s_add_u32 s57, s55, 20480\n\t"
    "buffer_store_dwordx4 v[100:103], v123, s[48:51], s57 offen sc1\n\t"
    "s_waitcnt lgkmcnt(3)\n\t"
    "v_mfma_f32_16x16x32_bf16 v[84:87], a[24:27], v[16:19], v[84:87]\n\t"
    "s_setprio 2\n\t"
    "s_waitcnt lgkmcnt(2)\n\t"
    "v_mfma_f32_16x16x32_bf16 v[84:87], a[56:59], v[20:23], v[84:87]\n\t"
    "v_mul_f32 v80, v80, v116\n\t"
    "v_mul_f32 v81, v81, v116\n\t"
    "s_waitcnt lgkmcnt(1)\n\t"
    "v_mfma_f32_16x16x32_bf16 v[84:87], a[88:91], v[24:27], v[84:87]\n\t"
    "v_mul_f32 v82, v82, v116\n\t"
    "v_mul_f32 v83, v83, v116\n\t"
    "s_waitcnt lgkmcnt(0)\n\t"
    "v_mfma_f32_16x16x32_bf16 v[84:87], a[120:123], v[28:31], v[84:87]\n\t"
    "v_cvt_pk_bf16_f32 v96, v80, v81\n\t"
    "v_cvt_pk_bf16_f32 v97, v82, v83\n\t"
    "v_mfma_f32_16x16x32_bf16 v[92:95], a[28:31], v[16:19], v[92:95]\n\t"
    "v_mul_f32 v88, v88, v116\n\t"
    "v_mul_f32 v89, v89, v116\n\t"
    "v_mfma_f32_16x16x32_bf16 v[92:95], a[60:63], v[20:23], v[92:95]\n\t"
    "v_mul_f32 v90, v90, v116\n\t"
    "v_mul_f32 v91, v91, v116\n\t"
    "v_mfma_f32_16x16x32_bf16 v[92:95], a[92:95], v[24:27], v[92:95]\n\t"
    "v_cvt_pk_bf16_f32 v98, v88, v89\n\t"
    "v_cvt_pk_bf16_f32 v99, v90, v91\n\t"
    "v_mfma_f32_16x16x32_bf16 v[92:95], a[124:127], v[28:31], v[92:95]\n\t"
    "v_permlane16_swap_b32 v96, v98\n\t"
    "v_permlane16_swap_b32 v97, v99\n\t"
    "s_setprio 1\n\t"
    "s_add_u32 s57, s55, 24576\n\t"
    "buffer_store_dwordx4 v[96:99], v123, s[48:51], s57 offen sc1\n\t"
    "v_mul_f32 v84, v84, v117\n\t"
    "v_mul_f32 v85, v85, v117\n\t"
    "v_mul_f32 v86, v86, v117\n\t"
    "v_mul_f32 v87, v87, v117\n\t"
    "v_cvt_pk_bf16_f32 v100, v84, v85\n\t"
    "v_cvt_pk_bf16_f32 v101, v86, v87\n\t"
    "v_mul_f32 v92, v92, v117\n\t"
    "v_mul_f32 v93, v93, v117\n\t"
    "v_mul_f32 v94, v94, v117\n\t"
    "v_mul_f32 v95, v95, v117\n\t"
    "v_cvt_pk_bf16_f32 v102, v92, v93\n\t"
    "v_cvt_pk_bf16_f32 v103, v94, v95\n\t"
    "s_nop 0\n\t"
    "v_permlane16_swap_b32 v100, v102\n\t"
    "v_permlane16_swap_b32 v101, v103\n\t"
    "s_add_u32 s57, s55, 28672\n\t"
    "buffer_store_dwordx4 v[100:103], v123, s[48:51], s57 offen sc1\n\t"
    "s_add_u32 s55, s55, 32768\n\t"
    "s_waitcnt vmcnt(8)\n\t"
    "s_setprio 0\n\t"
    "s_barrier\n\t"
    "ds_read_b128 v[0:3], v114 offset:0\n\t"
    "ds_read_b128 v[4:7], v114 offset:1024\n\t"
    "ds_read_b128 v[8:11], v114 offset:2048\n\t"
    "ds_read_b128 v[12:15], v114 offset:3072\n\t"
    "ds_read_b128 v[16:19], v114 offset:8192\n\t"
    "ds_read_b128 v[20:23], v114 offset:9216\n\t"
    "ds_read_b128 v[24:27], v114 offset:10240\n\t"
    "ds_read_b128 v[28:31], v114 offset:11264\n\t"
    "s_add_u32 m0, s65, s53\n\t"
    "s_nop 0\n\t"
    "buffer_load_dwordx4 v120, s[40:43], s54 offen lds\n\t"
    "s_add_u32 m0, m0, 0x400\n\t"
    "s_add_u32 s56, s54, 128\n\t"
    "buffer_load_dwordx4 v120, s[40:43], s56 offen lds\n\t"
    "s_add_u32 s54, s54, 0x2000\n\t"
    "s_add_u32 s53, s53, 0x2000\n\t"
    "s_and_b32 s53, s53, 0xffff\n\t"
    "s_add_u32 m0, s65, s53\n\t"
    "s_nop 0\n\t"
    "buffer_load_dwordx4 v120, s[40:43], s54 offen lds\n\t"
    "s_add_u32 m0, m0, 0x400\n\t"
    "s_add_u32 s56, s54, 128\n\t"
    "buffer_load_dwordx4 v120, s[40:43], s56 offen lds\n\t"
    "s_add_u32 s54, s54, 0x2000\n\t"
    "s_add_u32 s53, s53, 0x2000\n\t"
    "s_and_b32 s53, s53, 0xffff\n\t"
    "s_add_u32 m0, s65, s53\n\t"
    "s_nop 0\n\t"
    "buffer_load_dwordx4 v120, s[40:43], s54 offen lds\n\t"
    "s_add_u32 m0, m0, 0x400\n\t"
    "s_add_u32 s56, s54, 128\n\t"
    "buffer_load_dwordx4 v120, s[40:43], s56 offen lds\n\t"
    "s_add_u32 s54, s54, 0x2000\n\t"
    "s_add_u32 s53, s53, 0x2000\n\t"
    "s_and_b32 s53, s53, 0xffff\n\t"
    "s_add_u32 m0, s65, s53\n\t"
    "s_nop 0\n\t"
    "buffer_load_dwordx4 v120, s[40:43], s54 offen lds\n\t"
    "s_add_u32 m0, m0, 0x400\n\t"
    "s_add_u32 s56, s54, 128\n\t"
    "buffer_load_dwordx4 v120, s[40:43], s56 offen lds\n\t"
    "s_add_u32 s54, s54, 0x2000\n\t"
    "s_add_u32 s53, s53, 0x2000\n\t"
    "s_and_b32 s53, s53, 0xffff\n\t"
    "s_add_u32 m0, s65, s53\n\t"
    "s_nop 0\n\t"
    "buffer_load_dwordx4 v120, s[40:43], s54 offen lds\n\t"
    "s_waitcnt lgkmcnt(0)\n\t"
    "v_mfma_f32_16x16x32_bf16 v[64:67], a[0:3], v[0:3], 0\n\t"
    "s_setprio 1\n\t"
    "v_mfma_f32_16x16x32_bf16 v[64:67], a[8:11], v[4:7], v[64:67]\n\t"
    "v_mfma_f32_16x16x32_bf16 v[64:67], a[16:19], v[8:11], v[64:67]\n\t"
    "v_mfma_f32_16x16x32_bf16 v[64:67], a[24:27], v[12:15], v[64:67]\n\t"
    "v_mfma_f32_16x16x32_bf16 v[64:67], a[32:35], v[16:19], v[64:67]\n\t"
    "v_mfma_f32_16x16x32_bf16 v[64:67], a[40:43], v[20:23], v[64:67]\n\t"
    "v_mfma_f32_16x16x32_bf16 v[64:67], a[48:51], v[24:27], v[64:67]\n\t"
    "v_mfma_f32_16x16x32_bf16 v[64:67], a[56:59], v[28:31], v[64:67]\n\t"
    "v_mfma_f32_16x16x32_bf16 v[72:75], a[4:7], v[0:3], 0\n\t"
    "v_mfma_f32_16x16x32_bf16 v[72:75], a[12:15], v[4:7], v[72:75]\n\t"
    "v_mfma_f32_16x16x32_bf16 v[72:75], a[20:23], v[8:11], v[72:75]\n\t"
    "v_mfma_f32_16x16x32_bf16 v[72:75], a[28:31], v[12:15], v[72:75]\n\t"
    "v_mfma_f32_16x16x32_bf16 v[72:75], a[36:39], v[16:19], v[72:75]\n\t"
    "v_mfma_f32_16x16x32_bf16 v[72:75], a[44:47], v[20:23], v[72:75]\n\t"
    "v_mfma_f32_16x16x32_bf16 v[72:75], a[52:55], v[24:27], v[72:75]\n\t"
    "v_mfma_f32_16x16x32_bf16 v[72:75], a[60:63], v[28:31], v[72:75]\n\t"
    "s_setprio 0\n\t"
    "ds_read_b128 v[0:3], v114 offset:16384\n\t"
    "ds_read_b128 v[4:7], v114 offset:17408\n\t"
    "ds_read_b128 v[8:11], v114 offset:18432\n\t"
    "ds_read_b128 v[12:15], v114 offset:19456\n\t"
    "ds_read_b128 v[16:19], v114 offset:24576\n\t"
    "ds_read_b128 v[20:23], v114 offset:25600\n\t"
    "ds_read_b128 v[24:27], v114 offset:26624\n\t"
    "ds_read_b128 v[28:31], v114 offset:27648\n\t"
    "ds_read_b32 v104, v115 offset:0\n\t"
    "ds_read_b32 v105, v115 offset:256\n\t"
    "ds_read_b32 v106, v115 offset:512\n\t"
    "ds_read_b32 v107, v115 offset:768\n\t"
    "buffer_store_dwordx4 v[96:99], v121, s[48:51], s67 offen\n\t"
    "buffer_store_dwordx4 v[100:103], v121, s[48:51], s67 offen\n\t"
    "s_waitcnt lgkmcnt(4)\n\t"
    "v_mfma_f32_16x16x32_bf16 v[64:67], a[64:67], v[0:3], v[64:67]\n\t"
    "s_setprio 1\n\t"
    "v_mfma_f32_16x16x32_bf16 v[64:67], a[72:75], v[4:7], v[64:67]\n\t"
    "s_waitcnt lgkmcnt(2)\n\t"
    "v_add_f32 v108, v104, v105\n\t"
    "v_mfma_f32_16x16x32_bf16 v[64:67], a[80:83], v[8:11], v[64:67]\n\t"
    "s_waitcnt lgkmcnt(0)\n\t"
    "v_add_f32 v109, v106, v107\n\t"
    "v_mfma_f32_16x16x32_bf16 v[64:67], a[88:91], v[12:15], v[64:67]\n\t"
    "v_add_f32 v108, v108, v109\n\t"
    "v_mfma_f32_16x16x32_bf16 v[64:67], a[96:99], v[16:19], v[64:67]\n\t"
    "v_cmp_nlt_f32 vcc, s64, v108\n\t"
    "v_mfma_f32_16x16x32_bf16 v[64:67], a[104:107], v[20:23], v[64:67]\n\t"
    "v_cmp_nlt_f32_e64 s[62:63], v108, v122\n\t"
    "v_mfma_f32_16x16x32_bf16 v[64:67], a[112:115], v[24:27], v[64:67]\n\t"
    "v_rcp_f32 v110, v108\n\t"
    "v_mfma_f32_16x16x32_bf16 v[64:67], a[120:123], v[28:31], v[64:67]\n\t"
    "v_mov_b32 v111, v110\n\t"
    "v_mfma_f32_16x16x32_bf16 v[72:75], a[68:71], v[0:3], v[72:75]\n\t"
    "v_mfma_f32_16x16x32_bf16 v[72:75], a[76:79], v[4:7], v[72:75]\n\t"
    "v_permlane32_swap_b32 v110, v111\n\t"
    "v_mfma_f32_16x16x32_bf16 v[72:75], a[84:87], v[8:11], v[72:75]\n\t"
    "v_mfma_f32_16x16x32_bf16 v[72:75], a[92:95], v[12:15], v[72:75]\n\t"
    "v_mfma_f32_16x16x32_bf16 v[72:75], a[100:103], v[16:19], v[72:75]\n\t"
    "v_mfma_f32_16x16x32_bf16 v[72:75], a[108:111], v[20:23], v[72:75]\n\t"
    "v_mfma_f32_16x16x32_bf16 v[72:75], a[116:119], v[24:27], v[72:75]\n\t"
    "v_mfma_f32_16x16x32_bf16 v[72:75], a[124:127], v[28:31], v[72:75]\n\t"
    "s_setprio 0\n\t"
    "ds_read_b128 v[0:3], v114 offset:4096\n\t"
    "ds_read_b128 v[4:7], v114 offset:5120\n\t"
    "ds_read_b128 v[8:11], v114 offset:6144\n\t"
    "ds_read_b128 v[12:15], v114 offset:7168\n\t"
    "ds_read_b128 v[16:19], v114 offset:12288\n\t"
    "ds_read_b128 v[20:23], v114 offset:13312\n\t"
    "ds_read_b128 v[24:27], v114 offset:14336\n\t"
    "ds_read_b128 v[28:31], v114 offset:15360\n\t"
    "s_or_b64 s[62:63], s[62:63], vcc\n\t"
    "s_cmp_lg_u64 s[62:63], 0\n\t"
    "s_cselect_b64 s[62:63], s[60:61], 0\n\t"
    "s_or_b64 s[58:59], s[58:59], s[62:63]\n\t"
    "s_lshl_b64 s[60:61], s[60:61], 1\n\t"
    "s_waitcnt lgkmcnt(0)\n\t"
    "v_mfma_f32_16x16x32_bf16 v[68:71], a[0:3], v[0:3], 0\n\t"
    "s_setprio 1\n\t"
    "v_mfma_f32_16x16x32_bf16 v[68:71], a[8:11], v[4:7], v[68:71]\n\t"
    "v_mfma_f32_16x16x32_bf16 v[68:71], a[16:19], v[8:11], v[68:71]\n\t"
    "v_mfma_f32_16x16x32_bf16 v[68:71], a[24:27], v[12:15], v[68:71]\n\t"
    "v_mfma_f32_16x16x32_bf16 v[68:71], a[32:35], v[16:19], v[68:71]\n\t"
    "v_mfma_f32_16x16x32_bf16 v[68:71], a[40:43], v[20:23], v[68:71]\n\t"
    "v_mfma_f32_16x16x32_bf16 v[68:71], a[48:51], v[24:27], v[68:71]\n\t"
    "v_mfma_f32_16x16x32_bf16 v[68:71], a[56:59], v[28:31], v[68:71]\n\t"
    "v_mfma_f32_16x16x32_bf16 v[76:79], a[4:7], v[0:3], 0\n\t"
    "v_mfma_f32_16x16x32_bf16 v[76:79], a[12:15], v[4:7], v[76:79]\n\t"
    "v_mfma_f32_16x16x32_bf16 v[76:79], a[20:23], v[8:11], v[76:79]\n\t"
    "v_mfma_f32_16x16x32_bf16 v[76:79], a[28:31], v[12:15], v[76:79]\n\t"
    "v_mfma_f32_16x16x32_bf16 v[76:79], a[36:39], v[16:19], v[76:79]\n\t"
    "v_mfma_f32_16x16x32_bf16 v[76:79], a[44:47], v[20:23], v[76:79]\n\t"
    "v_mfma_f32_16x16x32_bf16 v[76:79], a[52:55], v[24:27], v[76:79]\n\t"
    "v_mfma_f32_16x16x32_bf16 v[76:79], a[60:63], v[28:31], v[76:79]\n\t"
    "s_setprio 0\n\t"
    "ds_read_b128 v[0:3], v114 offset:20480\n\t"
    "ds_read_b128 v[4:7], v114 offset:21504\n\t"
    "ds_read_b128 v[8:11], v114 offset:22528\n\t"
    "ds_read_b128 v[12:15], v114 offset:23552\n\t"
    "ds_read_b128 v[16:19], v114 offset:28672\n\t"
    "ds_read_b128 v[20:23], v114 offset:29696\n\t"
    "ds_read_b128 v[24:27], v114 offset:30720\n\t"
    "ds_read_b128 v[28:31], v114 offset:31744\n\t"
    "s_add_u32 m0, m0, 0x400\n\t"
    "s_add_u32 s56, s54, 128\n\t"
    "buffer_load_dwordx4 v120, s[40:43], s56 offen lds\n\t"
    "s_add_u32 s54, s54, 0x2000\n\t"
    "s_add_u32 s53, s53, 0x2000\n\t"
    "s_and_b32 s53, s53, 0xffff\n\t"
    "s_waitcnt vmcnt(10)\n\t"
    "s_waitcnt lgkmcnt(0)\n\t"
    "s_barrier\n\t"
    "v_mfma_f32_16x16x32_bf16 v[68:71], a[64:67], v[0:3], v[68:71]\n\t"
    "s_setprio 1\n\t"
    "v_mfma_f32_16x16x32_bf16 v[68:71], a[72:75], v[4:7], v[68:71]\n\t"
    "v_mul_f32 v64, v64, v110\n\t"
    "v_mfma_f32_16x16x32_bf16 v[68:71], a[80:83], v[8:11], v[68:71]\n\t"
    "v_mul_f32 v65, v65, v110\n\t"
    "v_mfma_f32_16x16x32_bf16 v[68:71], a[88:91], v[12:15], v[68:71]\n\t"
    "v_mul_f32 v66, v66, v110\n\t"
    "v_mfma_f32_16x16x32_bf16 v[68:71], a[96:99], v[16:19], v[68:71]\n\t"
    "v_mul_f32 v67, v67, v110\n\t"
    "v_mfma_f32_16x16x32_bf16 v[68:71], a[104:107], v[20:23], v[68:71]\n\t"
    "v_cvt_pk_bf16_f32 v96, v64, v65\n\t"
    "v_mfma_f32_16x16x32_bf16 v[68:71], a[112:115], v[24:27], v[68:71]\n\t"
    "v_cvt_pk_bf16_f32 v97, v66, v67\n\t"
    "v_mfma_f32_16x16x32_bf16 v[68:71], a[120:123], v[28:31], v[68:71]\n\t"
    "v_mfma_f32_16x16x32_bf16 v[76:79], a[68:71], v[0:3], v[76:79]\n\t"
    "v_mfma_f32_16x16x32_bf16 v[76:79], a[76:79], v[4:7], v[76:79]\n\t"
    "v_mfma_f32_16x16x32_bf16 v[76:79], a[84:87], v[8:11], v[76:79]\n\t"
    "v_mfma_f32_16x16x32_bf16 v[76:79], a[92:95], v[12:15], v[76:79]\n\t"
    "v_mfma_f32_16x16x32_bf16 v[76:79], a[100:103], v[16:19], v[76:79]\n\t"
    "v_mfma_f32_16x16x32_bf16 v[76:79], a[108:111], v[20:23], v[76:79]\n\t"
    "v_mfma_f32_16x16x32_bf16 v[76:79], a[116:119], v[24:27], v[76:79]\n\t"
    "v_mfma_f32_16x16x32_bf16 v[76:79], a[124:127], v[28:31], v[76:79]\n\t"
    "s_setprio 0\n\t"
    "ds_read_b128 v[0:3], v114 offset:32768\n\t"
    "ds_read_b128 v[4:7], v114 offset:33792\n\t"
    "ds_read_b128 v[8:11], v114 offset:34816\n\t"
    "ds_read_b128 v[12:15], v114 offset:35840\n\t"
    "ds_read_b128 v[16:19], v114 offset:40960\n\t"
    "ds_read_b128 v[20:23], v114 offset:41984\n\t"
    "ds_read_b128 v[24:27], v114 offset:43008\n\t"
    "ds_read_b128 v[28:31], v114 offset:44032\n\t"
    "s_add_u32 m0, s65, s53\n\t"
    "s_nop 0\n\t"
    "buffer_load_dwordx4 v120, s[40:43], s54 offen lds\n\t"
    "s_waitcnt lgkmcnt(0)\n\t"
    "v_mfma_f32_16x16x32_bf16 v[80:83], a[0:3], v[0:3], 0\n\t"
    "s_setprio 1\n\t"
    "v_mfma_f32_16x16x32_bf16 v[80:83], a[8:11], v[4:7], v[80:83]\n\t"
    "v_mul_f32 v72, v72, v110\n\t"
    "v_mfma_f32_16x16x32_bf16 v[80:83], a[16:19], v[8:11], v[80:83]\n\t"
    "v_mul_f32 v73, v73, v110\n\t"
    "v_mfma_f32_16x16x32_bf16 v[80:83], a[24:27], v[12:15], v[80:83]\n\t"
    "v_mul_f32 v74, v74, v110\n\t"
    "v_mfma_f32_16x16x32_bf16 v[80:83], a[32:35], v[16:19], v[80:83]\n\t"
    "v_mul_f32 v75, v75, v110\n\t"
    "v_mfma_f32_16x16x32_bf16 v[80:83], a[40:43], v[20:23], v[80:83]\n\t"
    "v_cvt_pk_bf16_f32 v98, v72, v73\n\t"
    "v_mfma_f32_16x16x32_bf16 v[80:83], a[48:51], v[24:27], v[80:83]\n\t"
    "v_cvt_pk_bf16_f32 v99, v74, v75\n\t"
    "v_mfma_f32_16x16x32_bf16 v[80:83], a[56:59], v[28:31], v[80:83]\n\t"
    "v_mfma_f32_16x16x32_bf16 v[88:91], a[4:7], v[0:3], 0\n\t"
    "v_permlane16_swap_b32 v96, v98\n\t"
    "v_mfma_f32_16x16x32_bf16 v[88:91], a[12:15], v[4:7], v[88:91]\n\t"
    "v_permlane16_swap_b32 v97, v99\n\t"
    "v_mfma_f32_16x16x32_bf16 v[88:91], a[20:23], v[8:11], v[88:91]\n\t"
    "v_mfma_f32_16x16x32_bf16 v[88:91], a[28:31], v[12:15], v[88:91]\n\t"
    "v_mfma_f32_16x16x32_bf16 v[88:91], a[36:39], v[16:19], v[88:91]\n\t"
    "v_mfma_f32_16x16x32_bf16 v[88:91], a[44:47], v[20:23], v[88:91]\n\t"
    "v_mfma_f32_16x16x32_bf16 v[88:91], a[52:55], v[24:27], v[88:91]\n\t"
    "v_mfma_f32_16x16x32_bf16 v[88:91], a[60:63], v[28:31], v[88:91]\n\t"
    "s_setprio 0\n\t"
    "ds_read_b128 v[0:3], v114 offset:49152\n\t"
    "ds_read_b128 v[4:7], v114 offset:50176\n\t"
    "ds_read_b128 v[8:11], v114 offset:51200\n\t"
    "ds_read_b128 v[12:15], v114 offset:52224\n\t"
    "ds_read_b128 v[16:19], v114 offset:57344\n\t"
    "ds_read_b128 v[20:23], v114 offset:58368\n\t"
    "ds_read_b128 v[24:27], v114 offset:59392\n\t"
    "ds_read_b128 v[28:31], v114 offset:60416\n\t"
    "ds_read_b32 v104, v115 offset:1024\n\t"
    "ds_read_b32 v105, v115 offset:1280\n\t"
    "ds_read_b32 v106, v115 offset:1536\n\t"
    "ds_read_b32 v107, v115 offset:1792\n\t"
    "s_add_u32 s57, s55, 0\n\t"
    "buffer_store_dwordx4 v[96:99], v123, s[48:51], s57 offen sc1\n\t"
    "s_waitcnt lgkmcnt(4)\n\t"
    "v_mfma_f32_16x16x32_bf16 v[80:83], a[64:67], v[0:3], v[80:83]\n\t"
    "s_setprio 1\n\t"
    "v_mfma_f32_16x16x32_bf16 v[80:83], a[72:75], v[4:7], v[80:83]\n\t"
    "v_mul_f32 v68, v68, v111\n\t"
    "v_mfma_f32_16x16x32_bf16 v[80:83], a[80:83], v[8:11], v[80:83]\n\t"
    "v_mul_f32 v69, v69, v111\n\t"
    "v_mfma_f32_16x16x32_bf16 v[80:83], a[88:91], v[12:15], v[80:83]\n\t"
    "v_mul_f32 v70, v70, v111\n\t"
    "v_mfma_f32_16x16x32_bf16 v[80:83], a[96:99], v[16:19], v[80:83]\n\t"
    "v_mul_f32 v71, v71, v111\n\t"
    "v_mfma_f32_16x16x32_bf16 v[80:83], a[104:107], v[20:23], v[80:83]\n\t"
    "v_cvt_pk_bf16_f32 v100, v68, v69\n\t"
    "v_mfma_f32_16x16x32_bf16 v[80:83], a[112:115], v[24:27], v[80:83]\n\t"
    "v_cvt_pk_bf16_f32 v101, v70, v71\n\t"
    "v_mfma_f32_16x16x32_bf16 v[80:83], a[120:123], v[28:31], v[80:83]\n\t"
    "s_waitcnt lgkmcnt(2)\n\t"
    "v_add_f32 v108, v104, v105\n\t"
    "v_mfma_f32_16x16x32_bf16 v[88:91], a[68:71], v[0:3], v[88:91]\n\t"
    "s_waitcnt lgkmcnt(0)\n\t"
    "v_add_f32 v109, v106, v107\n\t"
    "v_mfma_f32_16x16x32_bf16 v[88:91], a[76:79], v[4:7], v[88:91]\n\t"
    "v_add_f32 v108, v108, v109\n\t"
    "v_mfma_f32_16x16x32_bf16 v[88:91], a[84:87], v[8:11], v[88:91]\n\t"
    "v_cmp_nlt_f32 vcc, s64, v108\n\t"
    "v_mfma_f32_16x16x32_bf16 v[88:91], a[92:95], v[12:15], v[88:91]\n\t"
    "v_cmp_nlt_f32_e64 s[62:63], v108, v122\n\t"
    "v_mfma_f32_16x16x32_bf16 v[88:91], a[100:103], v[16:19], v[88:91]\n\t"
    "v_rcp_f32 v112, v108\n\t"
    "v_mfma_f32_16x16x32_bf16 v[88:91], a[108:111], v[20:23], v[88:91]\n\t"
    "v_mov_b32 v113, v112\n\t"
    "v_mfma_f32_16x16x32_bf16 v[88:91], a[116:119], v[24:27], v[88:91]\n\t"
    "v_mfma_f32_16x16x32_bf16 v[88:91], a[124:127], v[28:31], v[88:91]\n\t"
    "v_permlane32_swap_b32 v112, v113\n\t"
    "s_setprio 0\n\t"
    "ds_read_b128 v[0:3], v114 offset:36864\n\t"
    "ds_read_b128 v[4:7], v114 offset:37888\n\t"
    "ds_read_b128 v[8:11], v114 offset:38912\n\t"
    "ds_read_b128 v[12:15], v114 offset:39936\n\t"
    "ds_read_b128 v[16:19], v114 offset:45056\n\t"
    "ds_read_b128 v[20:23], v114 offset:46080\n\t"
    "ds_read_b128 v[24:27], v114 offset:47104\n\t"
    "ds_read_b128 v[28:31], v114 offset:48128\n\t"
    "s_or_b64 s[62:63], s[62:63], vcc\n\t"
    "s_cmp_lg_u64 s[62:63], 0\n\t"
    "s_cselect_b64 s[62:63], s[60:61], 0\n\t"
    "s_or_b64 s[58:59], s[58:59], s[62:63]\n\t"
    "s_lshl_b64 s[60:61], s[60:61], 1\n\t"
    "s_waitcnt lgkmcnt(0)\n\t"
    "v_mfma_f32_16x16x32_bf16 v[84:87], a[0:3], v[0:3], 0\n\t"
    "s_setprio 1\n\t"
    "v_mfma_f32_16x16x32_bf16 v[84:87], a[8:11], v[4:7], v[84:87]\n\t"
    "v_mul_f32 v76, v76, v111\n\t"
    "v_mfma_f32_16x16x32_bf16 v[84:87], a[16:19], v[8:11], v[84:87]\n\t"
    "v_mul_f32 v77, v77, v111\n\t"
    "v_mfma_f32_16x16x32_bf16 v[84:87], a[24:27], v[12:15], v[84:87]\n\t"
    "v_mul_f32 v78, v78, v111\n\t"
    "v_mfma_f32_16x16x32_bf16 v[84:87], a[32:35], v[16:19], v[84:87]\n\t"
    "v_mul_f32 v79, v79, v111\n\t"
    "v_mfma_f32_16x16x32_bf16 v[84:87], a[40:43], v[20:23], v[84:87]\n\t"
    "v_cvt_pk_bf16_f32 v102, v76, v77\n\t"
    "v_mfma_f32_16x16x32_bf16 v[84:87], a[48:51], v[24:27], v[84:87]\n\t"
    "v_cvt_pk_bf16_f32 v103, v78, v79\n\t"
    "v_mfma_f32_16x16x32_bf16 v[84:87], a[56:59], v[28:31], v[84:87]\n\t"
    "v_mfma_f32_16x16x32_bf16 v[92:95], a[4:7], v[0:3], 0\n\t"
    "v_permlane16_swap_b32 v100, v102\n\t"
    "v_mfma_f32_16x16x32_bf16 v[92:95], a[12:15], v[4:7], v[92:95]\n\t"
    "v_permlane16_swap_b32 v101, v103\n\t"
    "v_mfma_f32_16x16x32_bf16 v[92:95], a[20:23], v[8:11], v[92:95]\n\t"
    "v_mfma_f32_16x16x32_bf16 v[92:95], a[28:31], v[12:15], v[92:95]\n\t"
    "v_mfma_f32_16x16x32_bf16 v[92:95], a[36:39], v[16:19], v[92:95]\n\t"
    "v_mfma_f32_16x16x32_bf16 v[92:95], a[44:47], v[20:23], v[92:95]\n\t"
    "v_mfma_f32_16x16x32_bf16 v[92:95], a[52:55], v[24:27], v[92:95]\n\t"
    "v_mfma_f32_16x16x32_bf16 v[92:95], a[60:63], v[28:31], v[92:95]\n\t"
    "s_setprio 0\n\t"
    "ds_read_b128 v[0:3], v114 offset:53248\n\t"
    "ds_read_b128 v[4:7], v114 offset:54272\n\t"
    "ds_read_b128 v[8:11], v114 offset:55296\n\t"
    "ds_read_b128 v[12:15], v114 offset:56320\n\t"
    "ds_read_b128 v[16:19], v114 offset:61440\n\t"
    "ds_read_b128 v[20:23], v114 offset:62464\n\t"
    "ds_read_b128 v[24:27], v114 offset:63488\n\t"
    "ds_read_b128 v[28:31], v114 offset:64512\n\t"
    "s_add_u32 s57, s55, 4096\n\t"
    "buffer_store_dwordx4 v[100:103], v123, s[48:51], s57 offen sc1\n\t"
    "s_add_u32 s55, s55, 0x2000\n\t"
    "s_add_u32 m0, m0, 0x400\n\t"
    "s_add_u32 s56, s54, 128\n\t"
    "buffer_load_dwordx4 v120, s[40:43], s56 offen lds\n\t"
    "s_add_u32 s54, s54, 0x2000\n\t"
    "s_add_u32 s53, s53, 0x2000\n\t"
    "s_and_b32 s53, s53, 0xffff\n\t"
    "s_waitcnt vmcnt(12)\n\t"
    "s_waitcnt lgkmcnt(0)\n\t"
    "s_mov_b32 s52, 20\n\t"
    "Lpv_%=:\n\t"
    "s_barrier\n\t"
    "v_mfma_f32_16x16x32_bf16 v[84:87], a[64:67], v[0:3], v[84:87]\n\t"
    "s_setprio 1\n\t"
    "v_mfma_f32_16x16x32_bf16 v[84:87], a[72:75], v[4:7], v[84:87]\n\t"
    "v_mul_f32 v80, v80, v112\n\t"
    "v_mfma_f32_16x16x32_bf16 v[84:87], a[80:83], v[8:11], v[84:87]\n\t"
    "v_mul_f32 v81, v81, v112\n\t"
    "v_mfma_f32_16x16x32_bf16 v[84:87], a[88:91], v[12:15], v[84:87]\n\t"
    "v_mul_f32 v82, v82, v112\n\t"
    "v_mfma_f32_16x16x32_bf16 v[84:87], a[96:99], v[16:19], v[84:87]\n\t"
    "v_mul_f32 v83, v83, v112\n\t"
    "v_mfma_f32_16x16x32_bf16 v[84:87], a[104:107], v[20:23], v[84:87]\n\t"
    "v_cvt_pk_bf16_f32 v96, v80, v81\n\t"
    "v_mfma_f32_16x16x32_bf16 v[84:87], a[112:115], v[24:27], v[84:87]\n\t"
    "v_cvt_pk_bf16_f32 v97, v82, v83\n\t"
    "v_mfma_f32_16x16x32_bf16 v[84:87], a[120:123], v[28:31], v[84:87]\n\t"
    "v_mfma_f32_16x16x32_bf16 v[92:95], a[68:71], v[0:3], v[92:95]\n\t"
    "v_mfma_f32_16x16x32_bf16 v[92:95], a[76:79], v[4:7], v[92:95]\n\t"
    "v_mfma_f32_16x16x32_bf16 v[92:95], a[84:87], v[8:11], v[92:95]\n\t"
    "v_mfma_f32_16x16x32_bf16 v[92:95], a[92:95], v[12:15], v[92:95]\n\t"
    "v_mfma_f32_16x16x32_bf16 v[92:95], a[100:103], v[16:19], v[92:95]\n\t"
    "v_mfma_f32_16x16x32_bf16 v[92:95], a[108:111], v[20:23], v[92:95]\n\t"
    "v_mfma_f32_16x16x32_bf16 v[92:95], a[116:119], v[24:27], v[92:95]\n\t"
    "v_mfma_f32_16x16x32_bf16 v[92:95], a[124:127], v[28:31], v[92:95]\n\t"
    "s_setprio 0\n\t"
    "ds_read_b128 v[0:3], v114 offset:0\n\t"
    "ds_read_b128 v[4:7], v114 offset:1024\n\t"
    "ds_read_b128 v[8:11], v114 offset:2048\n\t"
    "ds_read_b128 v[12:15], v114 offset:3072\n\t"
    "ds_read_b128 v[16:19], v114 offset:8192\n\t"
    "ds_read_b128 v[20:23], v114 offset:9216\n\t"
    "ds_read_b128 v[24:27], v114 offset:10240\n\t"
    "ds_read_b128 v[28:31], v114 offset:11264\n\t"
    "s_add_u32 m0, s65, s53\n\t"
    "s_nop 0\n\t"
    "buffer_load_dwordx4 v120, s[40:43], s54 offen lds\n\t"
    "s_waitcnt lgkmcnt(0)\n\t"
    "v_mfma_f32_16x16x32_bf16 v[64:67], a[0:3], v[0:3], 0\n\t"
    "s_setprio 1\n\t"
    "v_mfma_f32_16x16x32_bf16 v[64:67], a[8:11], v[4:7], v[64:67]\n\t"
    "v_mul_f32 v88, v88, v112\n\t"
    "v_mfma_f32_16x16x32_bf16 v[64:67], a[16:19], v[8:11], v[64:67]\n\t"
    "v_mul_f32 v89, v89, v112\n\t"
    "v_mfma_f32_16x16x32_bf16 v[64:67], a[24:27], v[12:15], v[64:67]\n\t"
    "v_mul_f32 v90, v90, v112\n\t"
    "v_mfma_f32_16x16x32_bf16 v[64:67], a[32:35], v[16:19], v[64:67]\n\t"
    "v_mul_f32 v91, v91, v112\n\t"
    "v_mfma_f32_16x16x32_bf16 v[64:67], a[40:43], v[20:23], v[64:67]\n\t"
    "v_cvt_pk_bf16_f32 v98, v88, v89\n\t"
    "v_mfma_f32_16x16x32_bf16 v[64:67], a[48:51], v[24:27], v[64:67]\n\t"
    "v_cvt_pk_bf16_f32 v99, v90, v91\n\t"
    "v_mfma_f32_16x16x32_bf16 v[64:67], a[56:59], v[28:31], v[64:67]\n\t"
    "v_mfma_f32_16x16x32_bf16 v[72:75], a[4:7], v[0:3], 0\n\t"
    "v_permlane16_swap_b32 v96, v98\n\t"
    "v_mfma_f32_16x16x32_bf16 v[72:75], a[12:15], v[4:7], v[72:75]\n\t"
    "v_permlane16_swap_b32 v97, v99\n\t"
    "v_mfma_f32_16x16x32_bf16 v[72:75], a[20:23], v[8:11], v[72:75]\n\t"
    "v_mfma_f32_16x16x32_bf16 v[72:75], a[28:31], v[12:15], v[72:75]\n\t"
    "v_mfma_f32_16x16x32_bf16 v[72:75], a[36:39], v[16:19], v[72:75]\n\t"
    "v_mfma_f32_16x16x32_bf16 v[72:75], a[44:47], v[20:23], v[72:75]\n\t"
    "v_mfma_f32_16x16x32_bf16 v[72:75], a[52:55], v[24:27], v[72:75]\n\t"
    "v_mfma_f32_16x16x32_bf16 v[72:75], a[60:63], v[28:31], v[72:75]\n\t"
    "s_setprio 0\n\t"
    "ds_read_b128 v[0:3], v114 offset:16384\n\t"
    "ds_read_b128 v[4:7], v114 offset:17408\n\t"
    "ds_read_b128 v[8:11], v114 offset:18432\n\t"
    "ds_read_b128 v[12:15], v114 offset:19456\n\t"
    "ds_read_b128 v[16:19], v114 offset:24576\n\t"
    "ds_read_b128 v[20:23], v114 offset:25600\n\t"
    "ds_read_b128 v[24:27], v114 offset:26624\n\t"
    "ds_read_b128 v[28:31], v114 offset:27648\n\t"
    "ds_read_b32 v104, v115 offset:0\n\t"
    "ds_read_b32 v105, v115 offset:256\n\t"
    "ds_read_b32 v106, v115 offset:512\n\t"
    "ds_read_b32 v107, v115 offset:768\n\t"
    "s_add_u32 s57, s55, 0\n\t"
    "buffer_store_dwordx4 v[96:99], v123, s[48:51], s57 offen sc1\n\t"
    "s_waitcnt lgkmcnt(4)\n\t"
    "v_mfma_f32_16x16x32_bf16 v[64:67], a[64:67], v[0:3], v[64:67]\n\t"
    "s_setprio 1\n\t"
    "v_mfma_f32_16x16x32_bf16 v[64:67], a[72:75], v[4:7], v[64:67]\n\t"
    "v_mul_f32 v84, v84, v113\n\t"
    "v_mfma_f32_16x16x32_bf16 v[64:67], a[80:83], v[8:11], v[64:67]\n\t"
    "v_mul_f32 v85, v85, v113\n\t"
    "v_mfma_f32_16x16x32_bf16 v[64:67], a[88:91], v[12:15], v[64:67]\n\t"
    "v_mul_f32 v86, v86, v113\n\t"
    "v_mfma_f32_16x16x32_bf16 v[64:67], a[96:99], v[16:19], v[64:67]\n\t"
    "v_mul_f32 v87, v87, v113\n\t"
    "v_mfma_f32_16x16x32_bf16 v[64:67], a[104:107], v[20:23], v[64:67]\n\t"
    "v_cvt_pk_bf16_f32 v100, v84, v85\n\t"
    "v_mfma_f32_16x16x32_bf16 v[64:67], a[112:115], v[24:27], v[64:67]\n\t"
    "v_cvt_pk_bf16_f32 v101, v86, v87\n\t"
    "v_mfma_f32_16x16x32_bf16 v[64:67], a[120:123], v[28:31], v[64:67]\n\t"
    "s_waitcnt lgkmcnt(2)\n\t"
    "v_add_f32 v108, v104, v105\n\t"
    "v_mfma_f32_16x16x32_bf16 v[72:75], a[68:71], v[0:3], v[72:75]\n\t"
    "s_waitcnt lgkmcnt(0)\n\t"
    "v_add_f32 v109, v106, v107\n\t"
    "v_mfma_f32_16x16x32_bf16 v[72:75], a[76:79], v[4:7], v[72:75]\n\t"
    "v_add_f32 v108, v108, v109\n\t"
    "v_mfma_f32_16x16x32_bf16 v[72:75], a[84:87], v[8:11], v[72:75]\n\t"
    "v_cmp_nlt_f32 vcc, s64, v108\n\t"
    "v_mfma_f32_16x16x32_bf16 v[72:75], a[92:95], v[12:15], v[72:75]\n\t"
    "v_cmp_nlt_f32_e64 s[62:63], v108, v122\n\t"
    "v_mfma_f32_16x16x32_bf16 v[72:75], a[100:103], v[16:19], v[72:75]\n\t"
    "v_rcp_f32 v110, v108\n\t"
    "v_mfma_f32_16x16x32_bf16 v[72:75], a[108:111], v[20:23], v[72:75]\n\t"
    "v_mov_b32 v111, v110\n\t"
    "v_mfma_f32_16x16x32_bf16 v[72:75], a[116:119], v[24:27], v[72:75]\n\t"
    "v_mfma_f32_16x16x32_bf16 v[72:75], a[124:127], v[28:31], v[72:75]\n\t"
    "v_permlane32_swap_b32 v110, v111\n\t"
    "s_setprio 0\n\t"
    "ds_read_b128 v[0:3], v114 offset:4096\n\t"
    "ds_read_b128 v[4:7], v114 offset:5120\n\t"
    "ds_read_b128 v[8:11], v114 offset:6144\n\t"
    "ds_read_b128 v[12:15], v114 offset:7168\n\t"
    "ds_read_b128 v[16:19], v114 offset:12288\n\t"
    "ds_read_b128 v[20:23], v114 offset:13312\n\t"
    "ds_read_b128 v[24:27], v114 offset:14336\n\t"
    "ds_read_b128 v[28:31], v114 offset:15360\n\t"
    "s_or_b64 s[62:63], s[62:63], vcc\n\t"
    "s_cmp_lg_u64 s[62:63], 0\n\t"
    "s_cselect_b64 s[62:63], s[60:61], 0\n\t"
    "s_or_b64 s[58:59], s[58:59], s[62:63]\n\t"
    "s_lshl_b64 s[60:61], s[60:61], 1\n\t"
    "s_waitcnt lgkmcnt(0)\n\t"
    "v_mfma_f32_16x16x32_bf16 v[68:71], a[0:3], v[0:3], 0\n\t"
    "s_setprio 1\n\t"
    "v_mfma_f32_16x16x32_bf16 v[68:71], a[8:11], v[4:7], v[68:71]\n\t"
    "v_mul_f32 v92, v92, v113\n\t"
    "v_mfma_f32_16x16x32_bf16 v[68:71], a[16:19], v[8:11], v[68:71]\n\t"
    "v_mul_f32 v93, v93, v113\n\t"
    "v_mfma_f32_16x16x32_bf16 v[68:71], a[24:27], v[12:15], v[68:71]\n\t"
    "v_mul_f32 v94, v94, v113\n\t"
    "v_mfma_f32_16x16x32_bf16 v[68:71], a[32:35], v[16:19], v[68:71]\n\t"
    "v_mul_f32 v95, v95, v113\n\t"
    "v_mfma_f32_16x16x32_bf16 v[68:71], a[40:43], v[20:23], v[68:71]\n\t"
    "v_cvt_pk_bf16_f32 v102, v92, v93\n\t"
    "v_mfma_f32_16x16x32_bf16 v[68:71], a[48:51], v[24:27], v[68:71]\n\t"
    "v_cvt_pk_bf16_f32 v103, v94, v95\n\t"
    "v_mfma_f32_16x16x32_bf16 v[68:71], a[56:59], v[28:31], v[68:71]\n\t"
    "v_mfma_f32_16x16x32_bf16 v[76:79], a[4:7], v[0:3], 0\n\t"
    "v_permlane16_swap_b32 v100, v102\n\t"
    "v_mfma_f32_16x16x32_bf16 v[76:79], a[12:15], v[4:7], v[76:79]\n\t"
    "v_permlane16_swap_b32 v101, v103\n\t"
    "v_mfma_f32_16x16x32_bf16 v[76:79], a[20:23], v[8:11], v[76:79]\n\t"
    "v_mfma_f32_16x16x32_bf16 v[76:79], a[28:31], v[12:15], v[76:79]\n\t"
    "v_mfma_f32_16x16x32_bf16 v[76:79], a[36:39], v[16:19], v[76:79]\n\t"
    "v_mfma_f32_16x16x32_bf16 v[76:79], a[44:47], v[20:23], v[76:79]\n\t"
    "v_mfma_f32_16x16x32_bf16 v[76:79], a[52:55], v[24:27], v[76:79]\n\t"
    "v_mfma_f32_16x16x32_bf16 v[76:79], a[60:63], v[28:31], v[76:79]\n\t"
    "s_setprio 0\n\t"
    "ds_read_b128 v[0:3], v114 offset:20480\n\t"
    "ds_read_b128 v[4:7], v114 offset:21504\n\t"
    "ds_read_b128 v[8:11], v114 offset:22528\n\t"
    "ds_read_b128 v[12:15], v114 offset:23552\n\t"
    "ds_read_b128 v[16:19], v114 offset:28672\n\t"
    "ds_read_b128 v[20:23], v114 offset:29696\n\t"
    "ds_read_b128 v[24:27], v114 offset:30720\n\t"
    "ds_read_b128 v[28:31], v114 offset:31744\n\t"
    "s_add_u32 s57, s55, 4096\n\t"
    "buffer_store_dwordx4 v[100:103], v123, s[48:51], s57 offen sc1\n\t"
    "s_add_u32 s55, s55, 0x2000\n\t"
    "s_add_u32 m0, m0, 0x400\n\t"
    "s_add_u32 s56, s54, 128\n\t"
    "buffer_load_dwordx4 v120, s[40:43], s56 offen lds\n\t"
    "s_add_u32 s54, s54, 0x2000\n\t"
    "s_add_u32 s53, s53, 0x2000\n\t"
    "s_and_b32 s53, s53, 0xffff\n\t"
    "s_waitcnt vmcnt(14)\n\t"
    "s_waitcnt lgkmcnt(0)\n\t"
    "s_barrier\n\t"
    "v_mfma_f32_16x16x32_bf16 v[68:71], a[64:67], v[0:3], v[68:71]\n\t"
    "s_setprio 1\n\t"
    "v_mfma_f32_16x16x32_bf16 v[68:71], a[72:75], v[4:7], v[68:71]\n\t"
    "v_mul_f32 v64, v64, v110\n\t"
    "v_mfma_f32_16x16x32_bf16 v[68:71], a[80:83], v[8:11], v[68:71]\n\t"
    "v_mul_f32 v65, v65, v110\n\t"
    "v_mfma_f32_16x16x32_bf16 v[68:71], a[88:91], v[12:15], v[68:71]\n\t"
    "v_mul_f32 v66, v66, v110\n\t"
    "v_mfma_f32_16x16x32_bf16 v[68:71], a[96:99], v[16:19], v[68:71]\n\t"
    "v_mul_f32 v67, v67, v110\n\t"
    "v_mfma_f32_16x16x32_bf16 v[68:71], a[104:107], v[20:23], v[68:71]\n\t"
    "v_cvt_pk_bf16_f32 v96, v64, v65\n\t"
    "v_mfma_f32_16x16x32_bf16 v[68:71], a[112:115], v[24:27], v[68:71]\n\t"
    "v_cvt_pk_bf16_f32 v97, v66, v67\n\t"
    "v_mfma_f32_16x16x32_bf16 v[68:71], a[120:123], v[28:31], v[68:71]\n\t"
    "v_mfma_f32_16x16x32_bf16 v[76:79], a[68:71], v[0:3], v[76:79]\n\t"
    "v_mfma_f32_16x16x32_bf16 v[76:79], a[76:79], v[4:7], v[76:79]\n\t"
    "v_mfma_f32_16x16x32_bf16 v[76:79], a[84:87], v[8:11], v[76:79]\n\t"
    "v_mfma_f32_16x16x32_bf16 v[76:79], a[92:95], v[12:15], v[76:79]\n\t"
    "v_mfma_f32_16x16x32_bf16 v[76:79], a[100:103], v[16:19], v[76:79]\n\t"
    "v_mfma_f32_16x16x32_bf16 v[76:79], a[108:111], v[20:23], v[76:79]\n\t"
    "v_mfma_f32_16x16x32_bf16 v[76:79], a[116:119], v[24:27], v[76:79]\n\t"
    "v_mfma_f32_16x16x32_bf16 v[76:79], a[124:127], v[28:31], v[76:79]\n\t"
    "s_setprio 0\n\t"
    "ds_read_b128 v[0:3], v114 offset:32768\n\t"
    "ds_read_b128 v[4:7], v114 offset:33792\n\t"
    "ds_read_b128 v[8:11], v114 offset:34816\n\t"
    "ds_read_b128 v[12:15], v114 offset:35840\n\t"
    "ds_read_b128 v[16:19], v114 offset:40960\n\t"
    "ds_read_b128 v[20:23], v114 offset:41984\n\t"
    "ds_read_b128 v[24:27], v114 offset:43008\n\t"
    "ds_read_b128 v[28:31], v114 offset:44032\n\t"
    "s_add_u32 m0, s65, s53\n\t"
    "s_nop 0\n\t"
    "buffer_load_dwordx4 v120, s[40:43], s54 offen lds\n\t"
    "s_waitcnt lgkmcnt(0)\n\t"
    "v_mfma_f32_16x16x32_bf16 v[80:83], a[0:3], v[0:3], 0\n\t"
    "s_setprio 1\n\t"
    "v_mfma_f32_16x16x32_bf16 v[80:83], a[8:11], v[4:7], v[80:83]\n\t"
    "v_mul_f32 v72, v72, v110\n\t"
    "v_mfma_f32_16x16x32_bf16 v[80:83], a[16:19], v[8:11], v[80:83]\n\t"
    "v_mul_f32 v73, v73, v110\n\t"
    "v_mfma_f32_16x16x32_bf16 v[80:83], a[24:27], v[12:15], v[80:83]\n\t"
    "v_mul_f32 v74, v74, v110\n\t"
    "v_mfma_f32_16x16x32_bf16 v[80:83], a[32:35], v[16:19], v[80:83]\n\t"
    "v_mul_f32 v75, v75, v110\n\t"
    "v_mfma_f32_16x16x32_bf16 v[80:83], a[40:43], v[20:23], v[80:83]\n\t"
    "v_cvt_pk_bf16_f32 v98, v72, v73\n\t"
    "v_mfma_f32_16x16x32_bf16 v[80:83], a[48:51], v[24:27], v[80:83]\n\t"
    "v_cvt_pk_bf16_f32 v99, v74, v75\n\t"
    "v_mfma_f32_16x16x32_bf16 v[80:83], a[56:59], v[28:31], v[80:83]\n\t"
    "v_mfma_f32_16x16x32_bf16 v[88:91], a[4:7], v[0:3], 0\n\t"
    "v_permlane16_swap_b32 v96, v98\n\t"
    "v_mfma_f32_16x16x32_bf16 v[88:91], a[12:15], v[4:7], v[88:91]\n\t"
    "v_permlane16_swap_b32 v97, v99\n\t"
    "v_mfma_f32_16x16x32_bf16 v[88:91], a[20:23], v[8:11], v[88:91]\n\t"
    "v_mfma_f32_16x16x32_bf16 v[88:91], a[28:31], v[12:15], v[88:91]\n\t"
    "v_mfma_f32_16x16x32_bf16 v[88:91], a[36:39], v[16:19], v[88:91]\n\t"
    "v_mfma_f32_16x16x32_bf16 v[88:91], a[44:47], v[20:23], v[88:91]\n\t"
    "v_mfma_f32_16x16x32_bf16 v[88:91], a[52:55], v[24:27], v[88:91]\n\t"
    "v_mfma_f32_16x16x32_bf16 v[88:91], a[60:63], v[28:31], v[88:91]\n\t"
    "s_setprio 0\n\t"
    "ds_read_b128 v[0:3], v114 offset:49152\n\t"
    "ds_read_b128 v[4:7], v114 offset:50176\n\t"
    "ds_read_b128 v[8:11], v114 offset:51200\n\t"
    "ds_read_b128 v[12:15], v114 offset:52224\n\t"
    "ds_read_b128 v[16:19], v114 offset:57344\n\t"
    "ds_read_b128 v[20:23], v114 offset:58368\n\t"
    "ds_read_b128 v[24:27], v114 offset:59392\n\t"
    "ds_read_b128 v[28:31], v114 offset:60416\n\t"
    "ds_read_b32 v104, v115 offset:1024\n\t"
    "ds_read_b32 v105, v115 offset:1280\n\t"
    "ds_read_b32 v106, v115 offset:1536\n\t"
    "ds_read_b32 v107, v115 offset:1792\n\t"
    "s_add_u32 s57, s55, 0\n\t"
    "buffer_store_dwordx4 v[96:99], v123, s[48:51], s57 offen sc1\n\t"
    "s_waitcnt lgkmcnt(4)\n\t"
    "v_mfma_f32_16x16x32_bf16 v[80:83], a[64:67], v[0:3], v[80:83]\n\t"
    "s_setprio 1\n\t"
    "v_mfma_f32_16x16x32_bf16 v[80:83], a[72:75], v[4:7], v[80:83]\n\t"
    "v_mul_f32 v68, v68, v111\n\t"
    "v_mfma_f32_16x16x32_bf16 v[80:83], a[80:83], v[8:11], v[80:83]\n\t"
    "v_mul_f32 v69, v69, v111\n\t"
    "v_mfma_f32_16x16x32_bf16 v[80:83], a[88:91], v[12:15], v[80:83]\n\t"
    "v_mul_f32 v70, v70, v111\n\t"
    "v_mfma_f32_16x16x32_bf16 v[80:83], a[96:99], v[16:19], v[80:83]\n\t"
    "v_mul_f32 v71, v71, v111\n\t"
    "v_mfma_f32_16x16x32_bf16 v[80:83], a[104:107], v[20:23], v[80:83]\n\t"
    "v_cvt_pk_bf16_f32 v100, v68, v69\n\t"
    "v_mfma_f32_16x16x32_bf16 v[80:83], a[112:115], v[24:27], v[80:83]\n\t"
    "v_cvt_pk_bf16_f32 v101, v70, v71\n\t"
    "v_mfma_f32_16x16x32_bf16 v[80:83], a[120:123], v[28:31], v[80:83]\n\t"
    "s_waitcnt lgkmcnt(2)\n\t"
    "v_add_f32 v108, v104, v105\n\t"
    "v_mfma_f32_16x16x32_bf16 v[88:91], a[68:71], v[0:3], v[88:91]\n\t"
    "s_waitcnt lgkmcnt(0)\n\t"
    "v_add_f32 v109, v106, v107\n\t"
    "v_mfma_f32_16x16x32_bf16 v[88:91], a[76:79], v[4:7], v[88:91]\n\t"
    "v_add_f32 v108, v108, v109\n\t"
    "v_mfma_f32_16x16x32_bf16 v[88:91], a[84:87], v[8:11], v[88:91]\n\t"
    "v_cmp_nlt_f32 vcc, s64, v108\n\t"
    "v_mfma_f32_16x16x32_bf16 v[88:91], a[92:95], v[12:15], v[88:91]\n\t"
    "v_cmp_nlt_f32_e64 s[62:63], v108, v122\n\t"
    "v_mfma_f32_16x16x32_bf16 v[88:91], a[100:103], v[16:19], v[88:91]\n\t"
    "v_rcp_f32 v112, v108\n\t"
    "v_mfma_f32_16x16x32_bf16 v[88:91], a[108:111], v[20:23], v[88:91]\n\t"
    "v_mov_b32 v113, v112\n\t"
    "v_mfma_f32_16x16x32_bf16 v[88:91], a[116:119], v[24:27], v[88:91]\n\t"
    "v_mfma_f32_16x16x32_bf16 v[88:91], a[124:127], v[28:31], v[88:91]\n\t"
    "v_permlane32_swap_b32 v112, v113\n\t"
    "s_setprio 0\n\t"
    "ds_read_b128 v[0:3], v114 offset:36864\n\t"
    "ds_read_b128 v[4:7], v114 offset:37888\n\t"
    "ds_read_b128 v[8:11], v114 offset:38912\n\t"
    "ds_read_b128 v[12:15], v114 offset:39936\n\t"
    "ds_read_b128 v[16:19], v114 offset:45056\n\t"
    "ds_read_b128 v[20:23], v114 offset:46080\n\t"
    "ds_read_b128 v[24:27], v114 offset:47104\n\t"
    "ds_read_b128 v[28:31], v114 offset:48128\n\t"
    "s_or_b64 s[62:63], s[62:63], vcc\n\t"
    "s_cmp_lg_u64 s[62:63], 0\n\t"
    "s_cselect_b64 s[62:63], s[60:61], 0\n\t"
    "s_or_b64 s[58:59], s[58:59], s[62:63]\n\t"
    "s_lshl_b64 s[60:61], s[60:61], 1\n\t"
    "s_waitcnt lgkmcnt(0)\n\t"
    "v_mfma_f32_16x16x32_bf16 v[84:87], a[0:3], v[0:3], 0\n\t"
    "s_setprio 1\n\t"
    "v_mfma_f32_16x16x32_bf16 v[84:87], a[8:11], v[4:7], v[84:87]\n\t"
    "v_mul_f32 v76, v76, v111\n\t"
    "v_mfma_f32_16x16x32_bf16 v[84:87], a[16:19], v[8:11], v[84:87]\n\t"
    "v_mul_f32 v77, v77, v111\n\t"
    "v_mfma_f32_16x16x32_bf16 v[84:87], a[24:27], v[12:15], v[84:87]\n\t"
    "v_mul_f32 v78, v78, v111\n\t"
    "v_mfma_f32_16x16x32_bf16 v[84:87], a[32:35], v[16:19], v[84:87]\n\t"
    "v_mul_f32 v79, v79, v111\n\t"
    "v_mfma_f32_16x16x32_bf16 v[84:87], a[40:43], v[20:23], v[84:87]\n\t"
    "v_cvt_pk_bf16_f32 v102, v76, v77\n\t"
    "v_mfma_f32_16x16x32_bf16 v[84:87], a[48:51], v[24:27], v[84:87]\n\t"
    "v_cvt_pk_bf16_f32 v103, v78, v79\n\t"
    "v_mfma_f32_16x16x32_bf16 v[84:87], a[56:59], v[28:31], v[84:87]\n\t"
    "v_mfma_f32_16x16x32_bf16 v[92:95], a[4:7], v[0:3], 0\n\t"
    "v_permlane16_swap_b32 v100, v102\n\t"
    "v_mfma_f32_16x16x32_bf16 v[92:95], a[12:15], v[4:7], v[92:95]\n\t"
    "v_permlane16_swap_b32 v101, v103\n\t"
    "v_mfma_f32_16x16x32_bf16 v[92:95], a[20:23], v[8:11], v[92:95]\n\t"
    "v_mfma_f32_16x16x32_bf16 v[92:95], a[28:31], v[12:15], v[92:95]\n\t"
    "v_mfma_f32_16x16x32_bf16 v[92:95], a[36:39], v[16:19], v[92:95]\n\t"
    "v_mfma_f32_16x16x32_bf16 v[92:95], a[44:47], v[20:23], v[92:95]\n\t"
    "v_mfma_f32_16x16x32_bf16 v[92:95], a[52:55], v[24:27], v[92:95]\n\t"
    "v_mfma_f32_16x16x32_bf16 v[92:95], a[60:63], v[28:31], v[92:95]\n\t"
    "s_setprio 0\n\t"
    "ds_read_b128 v[0:3], v114 offset:53248\n\t"
    "ds_read_b128 v[4:7], v114 offset:54272\n\t"
    "ds_read_b128 v[8:11], v114 offset:55296\n\t"
    "ds_read_b128 v[12:15], v114 offset:56320\n\t"
    "ds_read_b128 v[16:19], v114 offset:61440\n\t"
    "ds_read_b128 v[20:23], v114 offset:62464\n\t"
    "ds_read_b128 v[24:27], v114 offset:63488\n\t"
    "ds_read_b128 v[28:31], v114 offset:64512\n\t"
    "s_add_u32 s57, s55, 4096\n\t"
    "buffer_store_dwordx4 v[100:103], v123, s[48:51], s57 offen sc1\n\t"
    "s_add_u32 s55, s55, 0x2000\n\t"
    "s_add_u32 m0, m0, 0x400\n\t"
    "s_add_u32 s56, s54, 128\n\t"
    "buffer_load_dwordx4 v120, s[40:43], s56 offen lds\n\t"
    "s_add_u32 s54, s54, 0x2000\n\t"
    "s_add_u32 s53, s53, 0x2000\n\t"
    "s_and_b32 s53, s53, 0xffff\n\t"
    "s_waitcnt vmcnt(16)\n\t"
    "s_waitcnt lgkmcnt(0)\n\t"
    "s_sub_u32 s52, s52, 1\n\t"
    "s_cmp_lg_u32 s52, 0\n\t"
    "s_cbranch_scc1 Lpv_%=\n\t"
    "s_barrier\n\t"
    "v_mfma_f32_16x16x32_bf16 v[84:87], a[64:67], v[0:3], v[84:87]\n\t"
    "s_setprio 1\n\t"
    "v_mfma_f32_16x16x32_bf16 v[84:87], a[72:75], v[4:7], v[84:87]\n\t"
    "v_mul_f32 v80, v80, v112\n\t"
    "v_mfma_f32_16x16x32_bf16 v[84:87], a[80:83], v[8:11], v[84:87]\n\t"
    "v_mul_f32 v81, v81, v112\n\t"
    "v_mfma_f32_16x16x32_bf16 v[84:87], a[88:91], v[12:15], v[84:87]\n\t"
    "v_mul_f32 v82, v82, v112\n\t"
    "v_mfma_f32_16x16x32_bf16 v[84:87], a[96:99], v[16:19], v[84:87]\n\t"
    "v_mul_f32 v83, v83, v112\n\t"
    "v_mfma_f32_16x16x32_bf16 v[84:87], a[104:107], v[20:23], v[84:87]\n\t"
    "v_cvt_pk_bf16_f32 v96, v80, v81\n\t"
    "v_mfma_f32_16x16x32_bf16 v[84:87], a[112:115], v[24:27], v[84:87]\n\t"
    "v_cvt_pk_bf16_f32 v97, v82, v83\n\t"
    "v_mfma_f32_16x16x32_bf16 v[84:87], a[120:123], v[28:31], v[84:87]\n\t"
    "v_mfma_f32_16x16x32_bf16 v[92:95], a[68:71], v[0:3], v[92:95]\n\t"
    "v_mfma_f32_16x16x32_bf16 v[92:95], a[76:79], v[4:7], v[92:95]\n\t"
    "v_mfma_f32_16x16x32_bf16 v[92:95], a[84:87], v[8:11], v[92:95]\n\t"
    "v_mfma_f32_16x16x32_bf16 v[92:95], a[92:95], v[12:15], v[92:95]\n\t"
    "v_mfma_f32_16x16x32_bf16 v[92:95], a[100:103], v[16:19], v[92:95]\n\t"
    "v_mfma_f32_16x16x32_bf16 v[92:95], a[108:111], v[20:23], v[92:95]\n\t"
    "v_mfma_f32_16x16x32_bf16 v[92:95], a[116:119], v[24:27], v[92:95]\n\t"
    "v_mfma_f32_16x16x32_bf16 v[92:95], a[124:127], v[28:31], v[92:95]\n\t"
    "s_setprio 0\n\t"
    "v_mul_f32 v88, v88, v112\n\t"
    "v_mul_f32 v89, v89, v112\n\t"
    "v_mul_f32 v90, v90, v112\n\t"
    "v_mul_f32 v91, v91, v112\n\t"
    "v_cvt_pk_bf16_f32 v98, v88, v89\n\t"
    "v_cvt_pk_bf16_f32 v99, v90, v91\n\t"
    "s_nop 0\n\t"
    "v_permlane16_swap_b32 v96, v98\n\t"
    "v_permlane16_swap_b32 v97, v99\n\t"
    "s_add_u32 s57, s55, 0\n\t"
    "buffer_store_dwordx4 v[96:99], v123, s[48:51], s57 offen sc1\n\t"
    "v_mul_f32 v84, v84, v113\n\t"
    "v_mul_f32 v85, v85, v113\n\t"
    "v_mul_f32 v86, v86, v113\n\t"
    "v_mul_f32 v87, v87, v113\n\t"
    "v_cvt_pk_bf16_f32 v100, v84, v85\n\t"
    "v_cvt_pk_bf16_f32 v101, v86, v87\n\t"
    "v_mul_f32 v92, v92, v113\n\t"
    "v_mul_f32 v93, v93, v113\n\t"
    "v_mul_f32 v94, v94, v113\n\t"
    "v_mul_f32 v95, v95, v113\n\t"
    "v_cvt_pk_bf16_f32 v102, v92, v93\n\t"
    "v_cvt_pk_bf16_f32 v103, v94, v95\n\t"
    "s_nop 0\n\t"
    "v_permlane16_swap_b32 v100, v102\n\t"
    "v_permlane16_swap_b32 v101, v103\n\t"
    "s_add_u32 s57, s55, 4096\n\t"
    "buffer_store_dwordx4 v[100:103], v123, s[48:51], s57 offen sc1\n\t"
    "s_add_u32 s55, s55, 0x2000\n\t"
    "s_add_u32 s56, s39, 149504\n\t"
    "v_mov_b32 v0, s56\n\t"
    "v_mov_b32 v2, s58\n\t"
    "v_mov_b32 v3, s59\n\t"
    "ds_or_b32 v0, v2\n\t"
    "ds_or_b32 v0, v3 offset:4\n\t"
    "s_waitcnt vmcnt(0)\n\t"
    "s_waitcnt lgkmcnt(0)\n\t"
    "s_barrier\n\t"
    "s_add_u32 s56, s39, 149504\n\t"
    "v_mov_b32 v0, s56\n\t"
    "ds_read_b64 v[2:3], v0\n\t"
    "s_waitcnt lgkmcnt(0)\n\t"
    "v_readfirstlane_b32 s58, v2\n\t"
    "v_readfirstlane_b32 s59, v3\n\t"
    "v_mbcnt_lo_u32_b32 v104, -1, 0\n\t"
    "v_mbcnt_hi_u32_b32 v104, -1, v104\n\t"
    "v_and_b32 v105, 15, v104\n\t"
    "v_lshrrev_b32 v104, 4, v104\n\t"
    "v_lshlrev_b32 v105, 8, v105\n\t"
    "v_lshl_add_u32 v105, v104, 3, v105\n\t"
    "s_lshl_b32 s57, s38, 6\n\t"
    "v_add_u32 v105, s57, v105\n\t"
    "LFBpv_%=:\n\t"
    "s_cmp_eq_u64 s[58:59], 0\n\t"
    "s_cbranch_scc1 LFBXpv_%=\n\t"
    "s_ff1_i32_b64 s68, s[58:59]\n\t"
    "s_bitset0_b64 s[58:59], s68\n\t"
    "s_lshl_b32 s55, s68, 13\n\t"
    "s_lshl_b32 s57, s38, 11\n\t"
    "s_add_u32 s54, s55, s57\n\t"
    "s_mov_b32 m0, s65\n\t"
    "s_nop 0\n\t"
    "buffer_load_dwordx4 v120, s[40:43], s54 offen lds\n\t"
    "s_add_u32 m0, m0, 0x400\n\t"
    "s_add_u32 s56, s54, 128\n\t"
    "buffer_load_dwordx4 v120, s[40:43], s56 offen lds\n\t"
    "s_waitcnt vmcnt(0)\n\t"
    "s_barrier\n\t"
    "s_barrier\n\t"
    "s_barrier\n\t"
    "s_barrier\n\t"
    "ds_read_b128 v[0:3], v114 offset:0\n\t"
    "ds_read_b128 v[4:7], v114 offset:1024\n\t"
    "ds_read_b128 v[8:11], v114 offset:2048\n\t"
    "ds_read_b128 v[12:15], v114 offset:3072\n\t"
    "ds_read_b128 v[16:19], v114 offset:8192\n\t"
    "ds_read_b128 v[20:23], v114 offset:9216\n\t"
    "ds_read_b128 v[24:27], v114 offset:10240\n\t"
    "ds_read_b128 v[28:31], v114 offset:11264\n\t"
    "s_waitcnt lgkmcnt(7)\n\t"
    "v_mfma_f32_16x16x32_bf16 v[64:67], a[0:3], v[0:3], 0\n\t"
    "s_waitcnt lgkmcnt(6)\n\t"
    "v_mfma_f32_16x16x32_bf16 v[64:67], a[8:11], v[4:7], v[64:67]\n\t"
    "s_waitcnt lgkmcnt(5)\n\t"
    "v_mfma_f32_16x16x32_bf16 v[64:67], a[16:19], v[8:11], v[64:67]\n\t"
    "s_waitcnt lgkmcnt(4)\n\t"
    "v_mfma_f32_16x16x32_bf16 v[64:67], a[24:27], v[12:15], v[64:67]\n\t"
    "s_waitcnt lgkmcnt(3)\n\t"
    "v_mfma_f32_16x16x32_bf16 v[64:67], a[32:35], v[16:19], v[64:67]\n\t"
    "s_waitcnt lgkmcnt(2)\n\t"
    "v_mfma_f32_16x16x32_bf16 v[64:67], a[40:43], v[20:23], v[64:67]\n\t"
    "s_waitcnt lgkmcnt(1)\n\t"
    "v_mfma_f32_16x16x32_bf16 v[64:67], a[48:51], v[24:27], v[64:67]\n\t"
    "s_waitcnt lgkmcnt(0)\n\t"
    "v_mfma_f32_16x16x32_bf16 v[64:67], a[56:59], v[28:31], v[64:67]\n\t"
    "v_mfma_f32_16x16x32_bf16 v[72:75], a[4:7], v[0:3], 0\n\t"
    "v_mfma_f32_16x16x32_bf16 v[72:75], a[12:15], v[4:7], v[72:75]\n\t"
    "v_mfma_f32_16x16x32_bf16 v[72:75], a[20:23], v[8:11], v[72:75]\n\t"
    "v_mfma_f32_16x16x32_bf16 v[72:75], a[28:31], v[12:15], v[72:75]\n\t"
    "v_mfma_f32_16x16x32_bf16 v[72:75], a[36:39], v[16:19], v[72:75]\n\t"
    "v_mfma_f32_16x16x32_bf16 v[72:75], a[44:47], v[20:23], v[72:75]\n\t"
    "v_mfma_f32_16x16x32_bf16 v[72:75], a[52:55], v[24:27], v[72:75]\n\t"
    "v_mfma_f32_16x16x32_bf16 v[72:75], a[60:63], v[28:31], v[72:75]\n\t"
    "ds_read_b128 v[0:3], v114 offset:16384\n\t"
    "ds_read_b128 v[4:7], v114 offset:17408\n\t"
    "ds_read_b128 v[8:11], v114 offset:18432\n\t"
    "ds_read_b128 v[12:15], v114 offset:19456\n\t"
    "ds_read_b128 v[16:19], v114 offset:24576\n\t"
    "ds_read_b128 v[20:23], v114 offset:25600\n\t"
    "ds_read_b128 v[24:27], v114 offset:26624\n\t"
    "ds_read_b128 v[28:31], v114 offset:27648\n\t"
    "s_waitcnt lgkmcnt(7)\n\t"
    "v_mfma_f32_16x16x32_bf16 v[64:67], a[64:67], v[0:3], v[64:67]\n\t"
    "s_waitcnt lgkmcnt(6)\n\t"
    "v_mfma_f32_16x16x32_bf16 v[64:67], a[72:75], v[4:7], v[64:67]\n\t"
    "s_waitcnt lgkmcnt(5)\n\t"
    "v_mfma_f32_16x16x32_bf16 v[64:67], a[80:83], v[8:11], v[64:67]\n\t"
    "s_waitcnt lgkmcnt(4)\n\t"
    "v_mfma_f32_16x16x32_bf16 v[64:67], a[88:91], v[12:15], v[64:67]\n\t"
    "s_waitcnt lgkmcnt(3)\n\t"
    "v_mfma_f32_16x16x32_bf16 v[64:67], a[96:99], v[16:19], v[64:67]\n\t"
    "s_waitcnt lgkmcnt(2)\n\t"
    "v_mfma_f32_16x16x32_bf16 v[64:67], a[104:107], v[20:23], v[64:67]\n\t"
    "s_waitcnt lgkmcnt(1)\n\t"
    "v_mfma_f32_16x16x32_bf16 v[64:67], a[112:115], v[24:27], v[64:67]\n\t"
    "s_waitcnt lgkmcnt(0)\n\t"
    "v_mfma_f32_16x16x32_bf16 v[64:67], a[120:123], v[28:31], v[64:67]\n\t"
    "v_mfma_f32_16x16x32_bf16 v[72:75], a[68:71], v[0:3], v[72:75]\n\t"
    "v_mfma_f32_16x16x32_bf16 v[72:75], a[76:79], v[4:7], v[72:75]\n\t"
    "v_mfma_f32_16x16x32_bf16 v[72:75], a[84:87], v[8:11], v[72:75]\n\t"
    "v_mfma_f32_16x16x32_bf16 v[72:75], a[92:95], v[12:15], v[72:75]\n\t"
    "v_mfma_f32_16x16x32_bf16 v[72:75], a[100:103], v[16:19], v[72:75]\n\t"
    "v_mfma_f32_16x16x32_bf16 v[72:75], a[108:111], v[20:23], v[72:75]\n\t"
    "v_mfma_f32_16x16x32_bf16 v[72:75], a[116:119], v[24:27], v[72:75]\n\t"
    "v_mfma_f32_16x16x32_bf16 v[72:75], a[124:127], v[28:31], v[72:75]\n\t"
    "ds_read_b128 v[0:3], v114 offset:4096\n\t"
    "ds_read_b128 v[4:7], v114 offset:5120\n\t"
    "ds_read_b128 v[8:11], v114 offset:6144\n\t"
    "ds_read_b128 v[12:15], v114 offset:7168\n\t"
    "ds_read_b128 v[16:19], v114 offset:12288\n\t"
    "ds_read_b128 v[20:23], v114 offset:13312\n\t"
    "ds_read_b128 v[24:27], v114 offset:14336\n\t"
    "ds_read_b128 v[28:31], v114 offset:15360\n\t"
    "s_waitcnt lgkmcnt(7)\n\t"
    "v_mfma_f32_16x16x32_bf16 v[68:71], a[0:3], v[0:3], 0\n\t"
    "s_waitcnt lgkmcnt(6)\n\t"
    "v_mfma_f32_16x16x32_bf16 v[68:71], a[8:11], v[4:7], v[68:71]\n\t"
    "s_waitcnt lgkmcnt(5)\n\t"
    "v_mfma_f32_16x16x32_bf16 v[68:71], a[16:19], v[8:11], v[68:71]\n\t"
    "s_waitcnt lgkmcnt(4)\n\t"
    "v_mfma_f32_16x16x32_bf16 v[68:71], a[24:27], v[12:15], v[68:71]\n\t"
    "s_waitcnt lgkmcnt(3)\n\t"
    "v_mfma_f32_16x16x32_bf16 v[68:71], a[32:35], v[16:19], v[68:71]\n\t"
    "s_waitcnt lgkmcnt(2)\n\t"
    "v_mfma_f32_16x16x32_bf16 v[68:71], a[40:43], v[20:23], v[68:71]\n\t"
    "s_waitcnt lgkmcnt(1)\n\t"
    "v_mfma_f32_16x16x32_bf16 v[68:71], a[48:51], v[24:27], v[68:71]\n\t"
    "s_waitcnt lgkmcnt(0)\n\t"
    "v_mfma_f32_16x16x32_bf16 v[68:71], a[56:59], v[28:31], v[68:71]\n\t"
    "v_mfma_f32_16x16x32_bf16 v[76:79], a[4:7], v[0:3], 0\n\t"
    "v_mfma_f32_16x16x32_bf16 v[76:79], a[12:15], v[4:7], v[76:79]\n\t"
    "v_mfma_f32_16x16x32_bf16 v[76:79], a[20:23], v[8:11], v[76:79]\n\t"
    "v_mfma_f32_16x16x32_bf16 v[76:79], a[28:31], v[12:15], v[76:79]\n\t"
    "v_mfma_f32_16x16x32_bf16 v[76:79], a[36:39], v[16:19], v[76:79]\n\t"
    "v_mfma_f32_16x16x32_bf16 v[76:79], a[44:47], v[20:23], v[76:79]\n\t"
    "v_mfma_f32_16x16x32_bf16 v[76:79], a[52:55], v[24:27], v[76:79]\n\t"
    "v_mfma_f32_16x16x32_bf16 v[76:79], a[60:63], v[28:31], v[76:79]\n\t"
    "ds_read_b128 v[0:3], v114 offset:20480\n\t"
    "ds_read_b128 v[4:7], v114 offset:21504\n\t"
    "ds_read_b128 v[8:11], v114 offset:22528\n\t"
    "ds_read_b128 v[12:15], v114 offset:23552\n\t"
    "ds_read_b128 v[16:19], v114 offset:28672\n\t"
    "ds_read_b128 v[20:23], v114 offset:29696\n\t"
    "ds_read_b128 v[24:27], v114 offset:30720\n\t"
    "ds_read_b128 v[28:31], v114 offset:31744\n\t"
    "s_waitcnt lgkmcnt(7)\n\t"
    "v_mfma_f32_16x16x32_bf16 v[68:71], a[64:67], v[0:3], v[68:71]\n\t"
    "s_waitcnt lgkmcnt(6)\n\t"
    "v_mfma_f32_16x16x32_bf16 v[68:71], a[72:75], v[4:7], v[68:71]\n\t"
    "s_waitcnt lgkmcnt(5)\n\t"
    "v_mfma_f32_16x16x32_bf16 v[68:71], a[80:83], v[8:11], v[68:71]\n\t"
    "s_waitcnt lgkmcnt(4)\n\t"
    "v_mfma_f32_16x16x32_bf16 v[68:71], a[88:91], v[12:15], v[68:71]\n\t"
    "s_waitcnt lgkmcnt(3)\n\t"
    "v_mfma_f32_16x16x32_bf16 v[68:71], a[96:99], v[16:19], v[68:71]\n\t"
    "s_waitcnt lgkmcnt(2)\n\t"
    "v_mfma_f32_16x16x32_bf16 v[68:71], a[104:107], v[20:23], v[68:71]\n\t"
    "s_waitcnt lgkmcnt(1)\n\t"
    "v_mfma_f32_16x16x32_bf16 v[68:71], a[112:115], v[24:27], v[68:71]\n\t"
    "s_waitcnt lgkmcnt(0)\n\t"
    "v_mfma_f32_16x16x32_bf16 v[68:71], a[120:123], v[28:31], v[68:71]\n\t"
    "v_mfma_f32_16x16x32_bf16 v[76:79], a[68:71], v[0:3], v[76:79]\n\t"
    "v_mfma_f32_16x16x32_bf16 v[76:79], a[76:79], v[4:7], v[76:79]\n\t"
    "v_mfma_f32_16x16x32_bf16 v[76:79], a[84:87], v[8:11], v[76:79]\n\t"
    "v_mfma_f32_16x16x32_bf16 v[76:79], a[92:95], v[12:15], v[76:79]\n\t"
    "v_mfma_f32_16x16x32_bf16 v[76:79], a[100:103], v[16:19], v[76:79]\n\t"
    "v_mfma_f32_16x16x32_bf16 v[76:79], a[108:111], v[20:23], v[76:79]\n\t"
    "v_mfma_f32_16x16x32_bf16 v[76:79], a[116:119], v[24:27], v[76:79]\n\t"
    "v_mfma_f32_16x16x32_bf16 v[76:79], a[124:127], v[28:31], v[76:79]\n\t"
    "s_add_u32 s56, s55, 4096\n\t"
    "v_cvt_pk_bf16_f32 v96, v64, v65\n\t"
    "v_cvt_pk_bf16_f32 v97, v66, v67\n\t"
    "buffer_store_dwordx2 v[96:97], v105, s[48:51], s55 offen offset:0\n\t"
    "v_cvt_pk_bf16_f32 v98, v72, v73\n\t"
    "v_cvt_pk_bf16_f32 v99, v74, v75\n\t"
    "buffer_store_dwordx2 v[98:99], v105, s[48:51], s55 offen offset:32\n\t"
    "v_cvt_pk_bf16_f32 v100, v68, v69\n\t"
    "v_cvt_pk_bf16_f32 v101, v70, v71\n\t"
    "buffer_store_dwordx2 v[100:101], v105, s[48:51], s56 offen offset:0\n\t"
    "v_cvt_pk_bf16_f32 v102, v76, v77\n\t"
    "v_cvt_pk_bf16_f32 v103, v78, v79\n\t"
    "buffer_store_dwordx2 v[102:103], v105, s[48:51], s56 offen offset:32\n\t"
    "s_waitcnt vmcnt(0)\n\t"
    "s_barrier\n\t"
    "s_branch LFBpv_%=\n\t"
    "LFBXpv_%=:\n\t"
    "LEND_%=:"
    :
    : "s"(qb), "s"(kb), "s"(vb), "s"(ob), "s"(wave), "s"(ldsb)
    :
      "v0", "v1", "v2", "v3", "v4", "v5", "v6", "v7", "v8", "v9", "v10", "v11", "v12", "v13", "v14", "v15", "v16", "v17", "v18", "v19", "v20", "v21", "v22", "v23",
      "v24", "v25", "v26", "v27", "v28", "v29", "v30", "v31", "v32", "v33", "v34", "v35", "v36", "v37", "v38", "v39", "v40", "v41", "v42", "v43", "v44", "v45", "v46", "v47",
      "v48", "v49", "v50", "v51", "v52", "v53", "v54", "v55", "v56", "v57", "v58", "v59", "v60", "v61", "v62", "v63", "v64", "v65", "v66", "v67", "v68", "v69", "v70", "v71",
      "v72", "v73", "v74", "v75", "v76", "v77", "v78", "v79", "v80", "v81", "v82", "v83", "v84", "v85", "v86", "v87", "v88", "v89", "v90", "v91", "v92", "v93", "v94", "v95",
      "v96", "v97", "v98", "v99", "v100", "v101", "v102", "v103", "v104", "v105", "v106", "v107", "v108", "v109", "v110", "v111", "v112", "v113", "v114", "v115", "v116", "v117", "v118", "v119",
      "v120", "v121", "v122", "v123", "v124", "v125", "v126", "v127", "a0", "a1", "a2", "a3", "a4", "a5", "a6", "a7", "a8", "a9", "a10", "a11", "a12", "a13", "a14", "a15",
      "a16", "a17", "a18", "a19", "a20", "a21", "a22", "a23", "a24", "a25", "a26", "a27", "a28", "a29", "a30", "a31", "a32", "a33", "a34", "a35", "a36", "a37", "a38", "a39",
      "a40", "a41", "a42", "a43", "a44", "a45", "a46", "a47", "a48", "a49", "a50", "a51", "a52", "a53", "a54", "a55", "a56", "a57", "a58", "a59", "a60", "a61", "a62", "a63",
      "a64", "a65", "a66", "a67", "a68", "a69", "a70", "a71", "a72", "a73", "a74", "a75", "a76", "a77", "a78", "a79", "a80", "a81", "a82", "a83", "a84", "a85", "a86", "a87",
      "a88", "a89", "a90", "a91", "a92", "a93", "a94", "a95", "a96", "a97", "a98", "a99", "a100", "a101", "a102", "a103", "a104", "a105", "a106", "a107", "a108", "a109", "a110", "a111",
      "a112", "a113", "a114", "a115", "a116", "a117", "a118", "a119", "a120", "a121", "a122", "a123", "a124", "a125", "a126", "a127", "s36", "s37", "s38", "s39", "s40", "s41", "s42", "s43",
      "s44", "s45", "s46", "s47", "s48", "s49", "s50", "s51", "s52", "s53", "s54", "s55", "s56", "s57", "s58", "s59", "s60", "s61", "s62", "s63", "s64", "s65", "s66", "s67",
      "s68", "s69", "s70", "s71", "s72", "s73", "s74", "s75", "s76", "s77", "s78", "s79", "s80", "s81", "s82", "s83", "s84", "s85", "s86", "s87", "s88", "s89", "s90", "s91",
      "s92", "s93", "s94", "s95",
      "vcc", "scc", "m0", "memory");
}

extern "C" void kernel_launch(void* const* d_in, const int* in_sizes, int n_in,
                              void* d_out, int out_size, void* d_ws, size_t ws_size,
                              hipStream_t stream) {
  standalone__gqa_softmax__near_ridge_kernel<<<256, 512, 0, stream>>>(
      (const char*)d_in[0], (const char*)d_in[1], (const char*)d_in[2], (char*)d_out);
}
```
